```python
import torch

def _softmax(x, axis=-1):
    return torch.softmax(x, dim=axis)
def _gelu(x):
    return torch.nn.functional.gelu(x, approximate="tanh")
def _silu(x):
    return torch.nn.functional.silu(x)
def _sigmoid(x):
    return torch.sigmoid(x)
def _layernorm(x, axis=-1, eps=1e-5):
    m = x.mean(dim=axis, keepdim=True)
    v = x.var(dim=axis, keepdim=True, unbiased=False)
    return (x - m) / torch.sqrt(v + eps)
def _scatter_add(tgt, idx, src, axis=0):
    return tgt.clone().index_add_(axis, idx, src)
def _segment_sum(src, seg, n):
    out = torch.zeros((n,) + src.shape[1:], dtype=src.dtype)
    return out.index_add_(0, seg, src)
def _pad(x, pw):
    flat = tuple(v for pair in reversed(pw) for v in pair)
    return torch.nn.functional.pad(x, flat)

kernel_name = 'parallel_sum__ssm_conv_s4__dense_activation_mish__prompt'

def setup_inputs():

    _w = (lambda a: (lambda t: t.to(torch.bfloat16) if t.is_floating_point() else t)(torch.as_tensor(a)))
    def _A_setup():
        B, L, D, N = 1, 2048, 640, 192
        A_re = -(lambda _x: torch.exp(torch.as_tensor(_x)))((lambda *s: torch.randn(*s, dtype=torch.bfloat16) * 3.2)(D, N))
        A_im = torch.pi * (lambda *s: torch.randn(*s, dtype=torch.bfloat16) * 0.05)(D, N)
        B_re = (lambda *s: torch.randn(*s, dtype=torch.bfloat16) * 0.1)(D, N)
        B_im = (lambda *s: torch.randn(*s, dtype=torch.bfloat16) * 0.1)(D, N)
        C_re = (lambda *s: torch.randn(*s, dtype=torch.bfloat16) * 0.1)(D, N)
        C_im = (lambda *s: torch.randn(*s, dtype=torch.bfloat16) * 0.1)(D, N)
        log_dt = (lambda *s: torch.randn(*s, dtype=torch.bfloat16) * 0.05)(D) * 0.5 - 4.0
        Dskip = (lambda *s: torch.randn(*s, dtype=torch.bfloat16) * 0.05)(D)
        return {
            "u": _w((lambda *s: torch.randn(*s, dtype=torch.bfloat16) * 0.05)(B, L, D)),
            "A_re": _w(A_re),
            "A_im": _w(A_im),
            "B_re": _w(B_re),
            "B_im": _w(B_im),
            "C_re": _w(C_re),
            "C_im": _w(C_im),
            "log_dt": _w(log_dt),
            "Dskip": _w(Dskip),
        }
    _A_in = _A_setup()

    def _B_setup():
        B, S, D, F = 1, 2048, 640, 1536
        return {
            "x": _w((lambda *s: torch.randn(*s, dtype=torch.bfloat16) * 0.05)(B, S, D)),
            "w1": _w((lambda *s: torch.randn(*s, dtype=torch.bfloat16) * 0.8)(D, F) * (D ** -0.5)),
            "b1": _w((lambda *s: torch.randn(*s, dtype=torch.bfloat16) * 0.4)(F)),
            "w2": _w((lambda *s: torch.randn(*s, dtype=torch.bfloat16) * 0.2)(F, D) * (F ** -0.5)),
        }
    _B_in = _B_setup()

    return {
        "A_u": _A_in["u"],
        "A_A_re": _A_in["A_re"],
        "A_A_im": _A_in["A_im"],
        "A_B_re": _A_in["B_re"],
        "A_B_im": _A_in["B_im"],
        "A_C_re": _A_in["C_re"],
        "A_C_im": _A_in["C_im"],
        "A_log_dt": _A_in["log_dt"],
        "A_Dskip": _A_in["Dskip"],
        "B_x": _B_in["x"],
        "B_w1": _B_in["w1"],
        "B_b1": _B_in["b1"],
        "B_w2": _B_in["w2"],
    }

def reference(A_u, A_A_re, A_A_im, A_B_re, A_B_im, A_C_re, A_C_im, A_log_dt, A_Dskip, B_x, B_w1, B_b1, B_w2):
    def _A_ref(u, A_re, A_im, B_re, B_im, C_re, C_im, log_dt, Dskip):
        Bsz, L, D = u.shape
        dt = torch.exp(log_dt)[:, None]
        A = A_re + 1j * A_im
        Bc = B_re + 1j * B_im
        Cc = C_re + 1j * C_im
        logdA = dt * A
        dB = dt * Bc
        l = (lambda x, dt: x.to(dt))(torch.arange(L), A_re.dtype)
        P = torch.exp(logdA[:, :, None] * l[None, None, :])
        K = torch.einsum("dn,dnl,dn->dl", Cc, P, dB).real
        L2 = 2 * L
        Kp = _pad(K, ((0, 0), (0, L)))
        up = (lambda x, *d: x.permute(*d))(u, 0, 2, 1)
        up = _pad(up, ((0, 0), (0, 0), (0, L)))
        Kf = (lambda x, axes=(-2,-1): torch.fft.rfft2(x.float(), dim=axes))(Kp[:, None, :], axes=(-2, -1))
        Uf = (lambda x, axes=(-2,-1): torch.fft.rfft2(x.float(), dim=axes))(up[:, :, None, :], axes=(-2, -1))
        Yf = Uf * Kf[None, :, :, :]
        y = (lambda x, s, axes=(-2,-1): torch.fft.irfft2(x, s=s, dim=axes).to(torch.bfloat16))(Yf, (1, L2), axes=(-2, -1))[:, :, 0, :L]
        y = (lambda x, *d: x.permute(*d))(y, 0, 2, 1) + u * Dskip[None, None, :]
        return y

    def _B_ref(x, w1, b1, w2):
        h = torch.einsum("bsd,df->bsf", x, w1) + b1
        sp = torch.log1p(torch.exp(-torch.abs(h))) + torch.where(h > 0, h, torch.zeros_like(h))
        act = h * torch.tanh(sp)
        return torch.einsum("bsf,fd->bsd", act, w2)

    x = A_u
    xn = _layernorm(x, axis=-1)
    return x + _A_ref(xn, A_A_re, A_A_im, A_B_re, A_B_im, A_C_re, A_C_im, A_log_dt, A_Dskip) + _B_ref(xn, B_w1, B_b1, B_w2)
```

```cpp
#include <hip/hip_runtime.h>
#include <stdint.h>
typedef unsigned short u16; typedef unsigned u32; typedef unsigned long long u64;
typedef __attribute__((ext_vector_type(2))) float f32x2;
typedef __attribute__((ext_vector_type(4))) float f32x4;
typedef __attribute__((ext_vector_type(16))) float f32x16;
typedef __attribute__((ext_vector_type(2))) __bf16 bf16x2;
typedef __attribute__((ext_vector_type(4))) __bf16 bf16x4;
typedef __attribute__((ext_vector_type(8))) __bf16 bf16x8;
typedef __attribute__((ext_vector_type(2))) _Float16 f16x2;
typedef __attribute__((ext_vector_type(8))) _Float16 f16x8;
typedef __attribute__((ext_vector_type(2))) u32 u32x2;
typedef __attribute__((ext_vector_type(3))) u32 u32x3;
typedef __attribute__((ext_vector_type(4))) u32 u32x4;
#define LAS __attribute__((address_space(3)))
#define GAS __attribute__((address_space(1)))
#define RLX_AGENT __ATOMIC_RELAXED, __HIP_MEMORY_SCOPE_AGENT
#define RLX_WG __ATOMIC_RELAXED, __HIP_MEMORY_SCOPE_WORKGROUP
#if defined(__HIP_DEVICE_COMPILE__)
#define NOPK __attribute__((target("no-packed-fp32-ops")))
#else
#define NOPK
#endif
#define DEV __device__ __forceinline__ NOPK
#define CFENCE() asm volatile("" ::: "memory")
#define DRAIN() asm volatile("s_waitcnt vmcnt(0)" ::: "memory")
#define LDRAIN() asm volatile("s_waitcnt lgkmcnt(0)" ::: "memory")
#define VMW(n) asm volatile("s_waitcnt vmcnt(%0)" :: "i"(n) : "memory")
#define SCHED() __builtin_amdgcn_sched_barrier(0)
#define PRIO(n) __builtin_amdgcn_s_setprio(n)
#define DMA_G 14
#define BWS 1
#define G2MAP 0
#define GATE 0
#define GATEW 0
#define PW7 1
#define PSLEEP 4

constexpr int L_ = 2048, D_ = 640, N_ = 192, F_ = 1536;
constexpr u32 WS_EPOCH = 0;
constexpr u32 WS_FT = 4096;
constexpr u32 WS_FW = WS_FT + 256 * 64;
constexpr u32 WS_FD = WS_FW + 128 * 64;
constexpr u32 WS_FC = WS_FD + 1280 * 64;
constexpr u32 WS_FA = WS_FC + 1024 * 64;
constexpr u32 WS_XNA = WS_FA + 512 * 64;
constexpr u32 WS_XNT = WS_XNA + L_ * D_ * 2;
constexpr u32 WS_W2F = WS_XNT + L_ * D_ * 2;
constexpr u32 WS_ACT = WS_W2F + F_ * D_ * 2;
constexpr u32 WS_YT = WS_ACT + L_ * F_ * 2;
constexpr u32 WS_END = WS_YT + L_ * D_ * 2;
constexpr u32 WS_STAMP = WS_END;
constexpr u32 OOB = 0x40000000u;
constexpr int KT_SZ = 8320, KC1 = 4160, X_SZ = 4608;
constexpr int LD_KT = 122880, LD_X = LD_KT + 3 * KT_SZ;
constexpr int LD_SYNC = LD_X + 3 * X_SZ;
constexpr int LDS_BYTES = 163840;
constexpr int LD_SP = 61440;
constexpr int LNS = 1296;
enum { SY_W1 = 0, SY_G1DONE, SY_W2RDY, SY_TAGLO, SY_TAGHI, SY_TAGOK, SY_KRDY, SY_CONV, SY_MISH0, SY_MISH1, SY_MISH2, SY_MISH3,
       SY_XRDY, SY_LN, SY_FAOK, SY_AISS, SY_GPART, SY_YST, SY_SP0, SY_SP1, SY_SP2, SY_SP3, SY_W1A };
constexpr u32 SPIN_CAP = 1u << 18;

struct Params {
  const u16 *x, *Are, *Aim, *Bre, *Bim, *Cre, *Cim, *ldt, *dsk, *w1, *b1, *w2;
  u16* out; char* ws;
};

DEV float asf(u32 v) { return __builtin_bit_cast(float, v); }
DEV u32 asu(float v) { return __builtin_bit_cast(u32, v); }
DEV float bflo(u32 v) { return asf(v << 16); }
DEV float bfhi(u32 v) { return asf(v & 0xffff0000u); }
DEV u32 pk2(float a, float b) { f32x2 v = {a, b}; return __builtin_bit_cast(u32, __builtin_convertvector(v, bf16x2)); }
DEV float rbf(float a) { return bflo(pk2(a, a)); }
struct cf { float x, y; };
DEV cf cmul(cf a, cf b) { cf r = {__builtin_fmaf(-a.y, b.y, a.x * b.x), __builtin_fmaf(a.y, b.x, a.x * b.y)}; return r; }
DEV cf cmulc(cf a, cf b) { cf r = {__builtin_fmaf(a.y, b.y, a.x * b.x), __builtin_fmaf(a.y, b.x, -(a.x * b.y))}; return r; }
DEV cf csq(cf a) { cf r = {__builtin_fmaf(-a.y, a.y, a.x * a.x), (a.x + a.x) * a.y}; return r; }
DEV cf cexp2r(float lr, float ph) {
  float e = __builtin_amdgcn_exp2f(lr); float f = __builtin_amdgcn_fractf(ph);
  cf r = {e * __builtin_amdgcn_cosf(f), e * __builtin_amdgcn_sinf(f)}; return r; }
DEV cf csel(bool h, cf a) { cf r = {h ? a.x : 1.f, h ? a.y : 0.f}; return r; }
DEV u32 toh(cf a) { f32x2 v = {a.x, a.y}; return __builtin_bit_cast(u32, __builtin_convertvector(v, f16x2)); }
DEV u32 tohc(cf a) { f32x2 v = {a.x, -a.y}; return __builtin_bit_cast(u32, __builtin_convertvector(v, f16x2)); }
DEV u32 hmul(u32 a_, u32 b_) {
  const f16x2 a = __builtin_bit_cast(f16x2, a_), b = __builtin_bit_cast(f16x2, b_), nb = -b;
  const f16x2 s = __builtin_shufflevector(nb, b, 1, 2);
  return __builtin_bit_cast(u32, __builtin_elementwise_fma(a.yy, s, a.xx * b)); }
DEV u32 hmulc(u32 a_, u32 b_) {
  const f16x2 a = __builtin_bit_cast(f16x2, a_), b = __builtin_bit_cast(f16x2, b_), nb = -b;
  const f16x2 s = __builtin_shufflevector(b, nb, 0, 3);
  return __builtin_bit_cast(u32, __builtin_elementwise_fma(a.yy, b.yx, a.xx * s)); }
template <int CTRL> DEV float dppf(float v) { return asf(__builtin_amdgcn_update_dpp(0, asu(v), CTRL, 0xf, 0xf, true)); }
DEV float red8(float v) { v += dppf<0xB1>(v); v += dppf<0x4E>(v); v += dppf<0x141>(v); return v; }
DEV float red_mod4(float v) {
  v += dppf<0x124>(v); v += dppf<0x128>(v);
  auto r = __builtin_amdgcn_permlane16_swap(asu(v), asu(v), false, false);
  v = asf(r[0]) + asf(r[1]);
  auto q = __builtin_amdgcn_permlane32_swap(asu(v), asu(v), false, false);
  return asf(q[0]) + asf(q[1]); }
DEV float max64(float v) {
  v = __builtin_fmaxf(v, dppf<0xB1>(v)); v = __builtin_fmaxf(v, dppf<0x4E>(v)); v = __builtin_fmaxf(v, dppf<0x141>(v)); v = __builtin_fmaxf(v, dppf<0x140>(v));
  auto r = __builtin_amdgcn_permlane16_swap(asu(v), asu(v), false, false);
  v = __builtin_fmaxf(asf(r[0]), asf(r[1]));
  auto q = __builtin_amdgcn_permlane32_swap(asu(v), asu(v), false, false);
  return __builtin_fmaxf(asf(q[0]), asf(q[1])); }
DEV bool wall(bool c) { return __builtin_amdgcn_ballot_w64(c) == ~0ull; }

DEV u32 lds_ld(LAS char* s, int w) { return __hip_atomic_load((LAS u32*)(s + LD_SYNC) + w, RLX_WG); }
DEV void lds_st(LAS char* s, int w, u32 v) { __hip_atomic_store((LAS u32*)(s + LD_SYNC) + w, v, RLX_WG); }
DEV void lds_wait(LAS char* s, int w, u32 want) {
#pragma unroll 1
  for (u32 n = 0; n < (1u << 22); ++n) { if (__builtin_amdgcn_readfirstlane(lds_ld(s, w)) >= want) break; __builtin_amdgcn_s_sleep(1); }
  CFENCE();
}
DEV void lds_post(LAS char* s, int w, int lane) {
  LDRAIN();
  if (lane == 0) (void)__hip_atomic_fetch_add((LAS u32*)(s + LD_SYNC) + w, 1u, RLX_WG);
  CFENCE();
}
DEV void lds_post_nw(LAS char* s, int w, int lane) {
  CFENCE();
  if (lane == 0) (void)__hip_atomic_fetch_add((LAS u32*)(s + LD_SYNC) + w, 1u, RLX_WG);
  CFENCE();
}
DEV u64 get_tag(LAS char* s) {
  lds_wait(s, SY_TAGOK, 1);
  return (u64)__builtin_amdgcn_readfirstlane(lds_ld(s, SY_TAGLO)) | ((u64)__builtin_amdgcn_readfirstlane(lds_ld(s, SY_TAGHI)) << 32);
}
DEV u64 ld_flag(char* ws, u32 off) { return __hip_atomic_load((GAS u64*)(ws + off), RLX_AGENT); }
DEV void poll_flags(char* ws, u32 off, u64 tag) {
#pragma unroll 1
  for (u32 n = 0; n < SPIN_CAP; ++n) {
    if (wall(ld_flag(ws, off) == tag)) break;
    __builtin_amdgcn_s_sleep(PSLEEP);
  }
  CFENCE();
}
template <int NL> DEV void polln(char* ws, u32 base, int lane, int nvalid, u64 tag) {
#pragma unroll 1
  for (u32 n = 0; n < SPIN_CAP; ++n) {
    bool ok = true;
#pragma unroll
    for (int k = 0; k < NL; ++k) { int i = lane + 64 * k; u64 v = ld_flag(ws, base + i * 64); ok &= (v == tag) | (i >= nvalid); }
    if (wall(ok)) break;
    __builtin_amdgcn_s_sleep(16);
  }
  CFENCE();
}
DEV bf16x4 trd(LAS char* p) { return __builtin_amdgcn_ds_read_tr16_b64_v4bf16((LAS bf16x4*)p); }
DEV bf16x8 cat(bf16x4 a, bf16x4 b) { return __builtin_shufflevector(a, b, 0, 1, 2, 3, 4, 5, 6, 7); }
DEV bf16x8 asb(u32x4 v) { return __builtin_bit_cast(bf16x8, v); }
DEV LAS char* lp(u32 a) { return (LAS char*)(size_t)a; }
#define MFMA16(a, b, c) __builtin_amdgcn_mfma_f32_16x16x32_bf16(a, b, c, 0, 0, 0)
#define MFMA32(a, b, c) __builtin_amdgcn_mfma_f32_32x32x16_bf16(a, b, c, 0, 0, 0)
#define MFMA32H(a, b, c) __builtin_amdgcn_mfma_f32_32x32x16_f16(a, b, c, 0, 0, 0)
#define BLD(voff, soff) __builtin_amdgcn_raw_buffer_load_b128(rs, (voff), (soff), 16)
#define BST(v, voff, soff) __builtin_amdgcn_raw_buffer_store_b128((v), rs, (voff) + (soff), 0, 16)

#define SET_FLAG(off) do { const u32x4 f_ = {(u32)tag, (u32)(tag >> 32), (u32)tag, (u32)(tag >> 32)}; BST(f_, l16 + (lane < 4 ? 0u : OOB), (off)); } while (0)

#define STAMP(i)

DEV void setm0(u32 v) { asm volatile("s_mov_b32 m0, %0\n\ts_nop 0" :: "s"(v) : "memory"); }
template <int OFF> DEV void glds(const void* sbase, u32 voff) {
  asm volatile("global_load_lds_dwordx4 %0, %1 offset:%2" :: "v"(voff), "s"(sbase), "i"(OFF) : "memory");
}
DEV void w1_dma(const char* wb, u32 smb, int first, int last, int lane) {
  u32 so[3];
#pragma unroll
  for (int r = 0; r < 3; ++r) { const int c = r * 64 + lane, row = c / 12, cc = c - row * 12; so[r] = row * 3072 + cc * 16 + 2048 - 1024 * r; }
#pragma unroll 1
  for (int i = first; i < last; i += 3) {
    const char* src = wb + i * 16 * 3072 - 2048;
    setm0(smb + i * 3072);
    glds<0>(src, so[0]); glds<1024>(src, so[1]); glds<2048>(src, so[2]);
  }
}
DEV u32x4 ldA(u32 a) { LAS u32* ap = (LAS u32*)lp(a); u32x4 v = {ap[0], ap[1], ap[2], ap[3]}; return v; }
DEV u32x4 ldB(u32 a) { return *(LAS u32x4*)lp(a); }
struct CAddr { u32 a[4]; u32 b; };
DEV CAddr caddr(u32 aB, u32 bB) {
  CAddr c;
#pragma unroll
  for (int i = 0; i < 4; ++i) { c.a[i] = aB + 1024 * (3 - i); asm("" : "+v"(c.a[i])); }
  c.b = bB; asm("" : "+v"(c.b));
  return c;
}
#define FA_(c, q) ldA((c).a[(q) >> 1] + (((q) & 1) ? 0 : 512))
#define FB_(c, q) ldB((c).b + 512 * (q))
DEV void conv_hi2(const CAddr& n1, const CAddr& n2, u32x4 (&fa)[8], u32x4 (&fb)[8], f32x4 (&y)[4]) {
#pragma unroll
  for (int q = 0; q < 8; ++q) {
#pragma unroll
    for (int pp = 7 - q; pp >= 0 && pp + q >= 4; --pp) {
      y[q + pp - 4] = MFMA16(asb(fa[q]), asb(fb[pp]), y[q + pp - 4]);
      if (pp == 7 - q) { SCHED(); fb[pp] = FB_(n1, pp); SCHED(); }
    }
    SCHED(); fa[q] = FA_(n1, q); SCHED();
  }
#pragma unroll
  for (int pp = 7; pp >= 0; --pp) {
#pragma unroll
    for (int q = (pp < 4 ? 4 - pp : 0); q <= 7 - pp; ++q) {
      y[q + pp - 4] = MFMA16(asb(fa[q]), asb(fb[pp]), y[q + pp - 4]);
      if (pp == (q < 4 ? 4 - q : 0)) { SCHED(); fa[q] = FA_(n2, q); SCHED(); }
    }
    SCHED(); fb[pp] = FB_(n2, pp); SCHED();
  }
}
DEV void conv_lo1(const CAddr& n, const u32x4 (&ca)[4], const u32x4 (&cb)[4], u32x4 (&na)[4], u32x4 (&nb)[4], f32x4 (&y)[4]) {
  int k = 0;
#pragma unroll
  for (int q = 0; q < 4; ++q)
#pragma unroll
    for (int pp = 3 - q; pp >= 0; --pp) {
      y[q + pp] = MFMA16(asb(ca[q]), asb(cb[pp]), y[q + pp]);
      SCHED();
      if (k < 8) { if (k & 1) nb[3 - (k >> 1)] = FB_(n, 3 - (k >> 1)); else na[k >> 1] = FA_(n, k >> 1); }
      ++k;
      SCHED();
    }
}
DEV u32x4 diag_frag(u32 dsk, int l15, int g) {
  const u32 v = (g == 2 + (l15 >> 3)) ? ((l15 & 1) ? dsk << 16 : dsk) : 0u;
  const int w = (l15 & 7) >> 1;
  u32x4 r = {w == 0 ? v : 0u, w == 1 ? v : 0u, w == 2 ? v : 0u, w == 3 ? v : 0u};
  return r;
}
DEV void store_y(const __amdgpu_buffer_rsrc_t rs, const f32x4 (&y)[4], u32 base, int l15, int g) {
#pragma unroll
  for (int t = 0; t < 4; t += 2) {
    const u32 a0 = pk2(y[t].x, y[t].y), a1 = pk2(y[t].z, y[t].w), b0 = pk2(y[t + 1].x, y[t + 1].y), b1 = pk2(y[t + 1].z, y[t + 1].w);
    auto s0 = __builtin_amdgcn_permlane16_swap(a0, b0, false, false);
    auto s1 = __builtin_amdgcn_permlane16_swap(a1, b1, false, false);
    const u32x4 o = {s0[0], s1[0], s0[1], s1[1]};
    BST(o, (t + (g & 1)) * 512 + (l15 >> 2) * 128 + (g >> 1) * 64 + (l15 & 3) * 16, base);
  }
}
DEV u32 gld2(const void* sbase, u32 voff) { u32 r; asm volatile("global_load_ushort %0, %1, %2" : "=&v"(r) : "v"(voff), "s"(sbase) : "memory"); return r; }

template <bool SW>
DEV void gemm2(const __amdgpu_buffer_rsrc_t rs, LAS char* sm, int wave, int lane, int rt2, int nch,
               u32x4 (&bw)[6][5], u32x4 (&af)[4][6], f32x4 (&res)[5]) {
  const int own = wave & 3;
  const u32 l16 = lane * 16;
  LAS char* dst = sm + wave * 15360 + l16;
  f32x4 acc[2][5];
#pragma unroll
  for (int st = 0; st < 4; ++st) {
#pragma unroll
    for (int k = 0; k < 6; ++k) {
#pragma unroll
      for (int n = 0; n < 5; ++n) {
        f32x4 z = {0, 0, 0, 0};
        acc[st & 1][n] = MFMA16(asb(bw[k][n]), asb(af[st][k]), k ? acc[st & 1][n] : z);
      }
      SCHED();
      if (st == 0) af[3][k] = BLD(l16 + (k & 3) * 1024, WS_ACT + ((rt2 * 4 + own) * 48 + wave * 6 + (k & 4)) * 1024);
      if (st > 0 && k < 5) *(LAS f32x4*)(dst + ((st - 1) * 5 + k) * 1024) = acc[(st - 1) & 1][k];
      if (st > 0 && k == 5) { if (SW) lds_post_nw(sm, SY_SP0 + own, lane); else if (st == 3) lds_post_nw(sm, SY_GPART, lane); }
      SCHED();
    }
  }
  if (SW) {
    lds_wait(sm, SY_CONV, nch + 1);
#pragma unroll
    for (int n = 0; n < 5; ++n) *(LAS f32x4*)(sm + LD_KT + own * 5120 + n * 1024 + l16) = acc[1][n];
    lds_post_nw(sm, SY_SP0 + own, lane);
  } else {
#pragma unroll
    for (int n = 0; n < 5; ++n) res[n] = acc[1][n];
  }
}

__global__ __launch_bounds__(512, 2) NOPK void fused(Params p) {
  extern __shared__ __attribute__((aligned(16))) char smem_[];
  LAS char* sm = (LAS char*)smem_;
  const u32 smb = (u32)(size_t)sm;
  const int b = __builtin_amdgcn_workgroup_id_x(), xcd = b & 7, j = b >> 3;
  const int tid = __builtin_amdgcn_workitem_id_x(), wave = __builtin_amdgcn_readfirstlane(tid >> 6), lane = tid & 63;
  const int g = lane >> 4, l15 = lane & 15, l31 = lane & 31, h = lane >> 5;
  const u32 l16 = lane * 16;
  const __amdgpu_buffer_rsrc_t rs = __builtin_amdgcn_make_buffer_rsrc(p.ws, 0, WS_END, 0x00020000);
  STAMP(0);
  const int rt2 = xcd * 4 + (j & 3), ct2 = j >> 2;
  const int rp = (xcd & 3) * 4 + (j & 3), cs = (xcd >> 2) * 8 + (j >> 2);
  const int lnb = xcd * 32 + j;
  const int c0 = xcd * 80 + (j >> 1) * 5 + ((j & 1) ? 3 : 0), nch = (j & 1) ? 2 : 3;
  const int own = wave & 3;
  const u32 fcoff = WS_FC + ((rt2 * 8 + wave) * 4 + (lane & 3)) * 64;
  const char* w1b = (const char*)p.w1 + cs * 192;
  u64 tag;
  f32x4 res[5];
  u32x4 bw[6][5], af[4][6];
#define LOAD_BW1(q) bw[(q) / 5][(q) % 5] = BLD(l16 + (((q) % 5) & 3) * 1024, WS_W2F + ((wave * 6 + (q) / 5) * 40 + ct2 * 5 + (((q) % 5) & 4)) * 1024)
#define LOAD_BW() do { _Pragma("unroll") for (int q = 0; q < 30; ++q) LOAD_BW1(q); SCHED(); } while (0)
#define LOAD_AF() do { _Pragma("unroll") for (int st = 0; st < 3; ++st) _Pragma("unroll") for (int k = 0; k < 6; ++k) \
    af[st][k] = BLD(l16 + (k & 3) * 1024, WS_ACT + ((rt2 * 4 + ((own + 1 + st) & 3)) * 48 + wave * 6 + (k & 4)) * 1024); SCHED(); } while (0)

  if (wave < 4) {
    const int gw = wave;
    u32 braw[3];
    if (gw < 2) {
      PRIO(3);
      const int row = lane & 3, c16 = lane >> 2;
      u32x4 xv[5];
#pragma unroll
      for (int i = 0; i < 5; ++i) xv[i] = *(const u32x4*)(p.x + (lnb * 8 + gw * 4 + row) * 640 + (16 * i + c16) * 8);
      if (tid < 32) lds_st(sm, tid, 0);
      LDRAIN();
      __builtin_amdgcn_s_barrier();
      SCHED();
      const bf16x2 ones = __builtin_bit_cast(bf16x2, 0x3F803F80u);
      float s2[2] = {0.f, 0.f}, q2[2] = {0.f, 0.f};
#pragma unroll
      for (int i = 0; i < 5; ++i)
#pragma unroll
        for (int k = 0; k < 4; ++k) {
          const u32 xw = xv[i][k]; const bf16x2 v = __builtin_bit_cast(bf16x2, xw);
          s2[k & 1] = __builtin_amdgcn_fdot2_f32_bf16(v, ones, s2[k & 1], false);
          q2[k & 1] = __builtin_amdgcn_fdot2_f32_bf16(v, v, q2[k & 1], false);
        }
      const float mean = red_mod4(s2[0] + s2[1]) * (1.f / 640.f);
      const float var = __builtin_fmaxf(__builtin_fmaf(-mean, mean, red_mod4(q2[0] + q2[1]) * (1.f / 640.f)), 0.f);
      const float sd = rbf(__builtin_amdgcn_sqrtf(rbf(rbf(var) + 1e-5f)));
      const float rinv = __builtin_amdgcn_rcpf(sd), nmr = -rbf(mean) * rinv;
      LAS char* sc = sm + LD_X;
#pragma unroll
      for (int i = 0; i < 5; ++i) {
        u32x4 o;
#pragma unroll
        for (int k = 0; k < 4; ++k) o[k] = pk2(__builtin_fmaf(bflo(xv[i][k]), rinv, nmr), __builtin_fmaf(bfhi(xv[i][k]), rinv, nmr));
        BST(o, l16 + i * 1024, WS_XNA + (lnb * 2 + gw) * 5120);
        xv[i] = o;
      }
      STAMP(1);
      tag = get_tag(sm);
      DRAIN();
      SET_FLAG(WS_FA + (lnb * 2 + gw) * 64);
      STAMP(2);
#pragma unroll
      for (int i = 0; i < 5; ++i) *(LAS u32x4*)(sc + (gw * 4 + row) * LNS + (16 * i + c16) * 16) = xv[i];
      lds_post_nw(sm, SY_LN, lane);
#pragma unroll
      for (int n = 0; n < 3; ++n) braw[n] = p.b1[cs * 96 + n * 32 + l31];
    } else {
      w1_dma(w1b, smb, gw - 2, gw - 1, lane);
      __builtin_amdgcn_s_barrier();
      w1_dma(w1b, smb, gw + 1, 40, lane);
      STAMP(1);
      VMW(18);
      lds_post_nw(sm, SY_W1A, lane);
#pragma unroll
      for (int n = 0; n < 3; ++n) braw[n] = p.b1[cs * 96 + n * 32 + l31];
      DRAIN();
      lds_post_nw(sm, SY_W1, lane);
      STAMP(2);
      tag = get_tag(sm);
    }
    poll_flags(p.ws, WS_FA + (rp * 32 + gw * 8 + (lane & 7)) * 64, tag);
    lds_post_nw(sm, SY_FAOK, lane);
    PRIO(0);
    STAMP(3);
    u32x4 a[40];
    const u32 av = (l31 >> 2) * 5120 + h * 64 + (l31 & 3) * 16;
#define LOAD_A(t) a[t] = BLD(av + ((t) & 31) * 128, WS_XNA + (rp * 32 + gw * 8) * 5120 + ((t) >> 5) * 4096)
#pragma unroll
    for (int t = 0; t < 32; ++t) LOAD_A(t);
    SCHED();
    lds_wait(sm, SY_W1A, 3);
    PRIO(2);
    f32x16 acc[3];
    {
      u32 wb[2];
      wb[0] = smb + 192 * (8 * h + (l15 >> 2)) + 32 * (g & 1) + 8 * (lane & 3); wb[1] = wb[0] + 20 * 3072;
      asm("" : "+v"(wb[0]), "+v"(wb[1]));
      bf16x8 wf[3][3];
#define RDW(t, n) wf[(t) % 3][n] = cat(trd(lp(wb[(t) / 20] + ((t) % 20) * 3072 + (n) * 64)), trd(lp(wb[(t) / 20] + ((t) % 20) * 3072 + (n) * 64 + 768)))
#pragma unroll
      for (int t = 0; t < 2; ++t) { RDW(t, 0); RDW(t, 1); RDW(t, 2); }
      const u32x4 xe = {h ? 0u : 0x3F80u, 0u, 0u, 0u};
#pragma unroll
      for (int n = 0; n < 3; ++n) {
        f32x16 z;
#pragma unroll
        for (int i = 0; i < 16; ++i) z[i] = 0.f;
        const u32x4 we = {braw[n], 0u, 0u, 0u};
        acc[n] = MFMA32(asb(we), asb(xe), z);
      }
      SCHED();
#pragma unroll
      for (int t = 0; t < 40; ++t) {
#pragma unroll
        for (int n = 0; n < 3; ++n) {
          acc[n] = MFMA32(wf[t % 3][n], asb(a[t]), acc[n]);
          SCHED();
          if (t == 19 && n == 0) lds_wait(sm, SY_W1, 3);
          if (t + 2 < 40) RDW(t + 2, n);
          if (n == 0 && t >= 1 && t <= 8) LOAD_A(31 + t);
          if (n == 1 && t == 8) lds_post_nw(sm, SY_AISS, lane);
          SCHED();
        }
      }
    }
    lds_post_nw(sm, SY_G1DONE, lane);
    STAMP(4);
    {
      const u32 ao = (l31 >> 4) * 48 * 1024 + (h * 16 + l15) * 16;
      lds_wait(sm, SY_W2RDY, 1);
#pragma unroll
      for (int n = 0; n < 3; ++n)
#pragma unroll
        for (int c2 = 0; c2 < 2; ++c2) {
          float mv[8];
#pragma unroll
          for (int e = 0; e < 8; ++e) {
            const float hv = acc[n][8 * c2 + e];
            const float ex = __builtin_amdgcn_exp2f(hv * 1.44269504089f);
            const float rc = __builtin_amdgcn_rcpf(__builtin_fmaf(ex, ex + 2.f, 2.f));
            mv[e] = hv * __builtin_fmaf(rc, -2.f, 1.f);
            if ((e & 3) == 3) {
              const int q = ((n * 2 + c2) * 2 + (e >> 2)) * BWS;
              SCHED();
#pragma unroll
              for (int u = 0; u < BWS; ++u) if (q + u < 10 * BWS) LOAD_BW1(q + u);
              SCHED();
            }
          }
          u32x4 o = {pk2(mv[0], mv[1]), pk2(mv[2], mv[3]), pk2(mv[4], mv[5]), pk2(mv[6], mv[7])};
          BST(o, ao + n * 1024 + c2 * 512, WS_ACT + ((rp * 8 + gw * 2) * 48 + cs * 3) * 1024);
        }
    }
    lds_post_nw(sm, SY_MISH0 + gw, lane);
    STAMP(5);
    DRAIN();
    SET_FLAG(WS_FC + (((rp * 2 + (gw >> 1)) * 8 + (cs >> 1)) * 4 + (cs & 1) * 2 + (gw & 1)) * 64);
    PRIO(0);
    STAMP(6);
#pragma unroll
    for (int q = 10 * BWS; q < 30; ++q) LOAD_BW1(q);
    poll_flags(p.ws, fcoff, tag);
    STAMP(7);
    LOAD_AF();
    lds_wait(sm, SY_G1DONE, 4);
    gemm2<false>(rs, sm, wave, lane, rt2, nch, bw, af, res);
    STAMP(11);
    const u32 fdoff = WS_FD + ((rt2 >> 4) * 640 + ct2 * 80 + gw * 20 + (lane < 20 ? lane : 0)) * 64;
    const u64 fd = ld_flag(p.ws, fdoff);
    int orow[3], och[3];
    u32x4 xres[3];
#pragma unroll
    for (int i = 0; i < 3; ++i) {
      const int id = (i < 2 || lane < 32) ? lane + 64 * i : lane;
      orow[i] = (id * 205) >> 11; och[i] = id - orow[i] * 10;
      xres[i] = *(const u32x4*)(p.x + (rt2 * 64 + gw * 16 + orow[i]) * 640 + ct2 * 80 + och[i] * 8);
    }
    lds_wait(sm, SY_GPART, 4);
#pragma unroll
    for (int o = 1; o < 4; ++o)
#pragma unroll
      for (int n = 0; n < 5; ++n) res[n] += *(LAS f32x4*)(sm + ((gw - o) & 3) * 15360 + ((o - 1) * 5 + n) * 1024 + l16);
    if (!wall(fd == tag)) poll_flags(p.ws, fdoff, tag);
    CFENCE();
    STAMP(12);
    const u32 yo = (lane >> 3) * 4096 + (lane & 7) * 16;
    u32x4 yv[3];
#pragma unroll
    for (int i = 0; i < 3; ++i) yv[i] = BLD(yo + ((i == 2 && lane >= 32) ? OOB : 0u), WS_YT + (ct2 * 80 + gw * 20 + 8 * i) * 4096 + rt2 * 128);
    lds_wait(sm, SY_CONV, nch + 1);
#pragma unroll
    for (int i = 0; i < 3; ++i) if (i < 2 || lane < 32) *(LAS u32x4*)(sm + LD_X + gw * 2560 + i * 1024 + l16) = yv[i];
    lds_post(sm, SY_YST, lane);
#pragma unroll
    for (int st = 0; st < 3; ++st) {
      const int v = (gw - 1 - st) & 3;
      lds_wait(sm, SY_SP0 + v, st + 1);
#pragma unroll
      for (int n = 0; n < 5; ++n) res[n] += *(LAS f32x4*)(sm + LD_SP + v * 15360 + (st * 5 + n) * 1024 + l16);
    }
    lds_wait(sm, SY_YST, 4);
#pragma unroll
    for (int n = 0; n < 5; ++n) {
      bf16x4 yy = trd(sm + LD_X + (n * 16 + 4 * g + (l15 >> 2)) * 128 + ((lane & 3) >> 1) * 64 + gw * 16 + (lane & 1) * 8);
      u32x2 yu = __builtin_bit_cast(u32x2, yy);
      res[n] += f32x4{bflo(yu.x), bfhi(yu.x), bflo(yu.y), bfhi(yu.y)};
    }
    STAMP(8);
    lds_wait(sm, SY_SP0 + gw, 4);
    STAMP(9);
    LAS char* stg = sm + gw * 15360;
#pragma unroll
    for (int n = 0; n < 5; ++n) *(LAS f32x4*)(stg + l15 * 336 + n * 64 + g * 16) = res[n] + *(LAS f32x4*)(sm + LD_KT + gw * 5120 + n * 1024 + l16);
    CFENCE();
#pragma unroll
    for (int i = 0; i < 3; ++i) {
      f32x4 u = *(LAS f32x4*)(stg + orow[i] * 336 + och[i] * 32), v = *(LAS f32x4*)(stg + orow[i] * 336 + och[i] * 32 + 16);
      u32x4 o = {pk2(u.x + bflo(xres[i].x), u.y + bfhi(xres[i].x)), pk2(u.z + bflo(xres[i].y), u.w + bfhi(xres[i].y)),
                 pk2(v.x + bflo(xres[i].z), v.y + bfhi(xres[i].z)), pk2(v.z + bflo(xres[i].w), v.w + bfhi(xres[i].w))};
      if (i < 2 || lane < 32) *(u32x4*)(p.out + (rt2 * 64 + gw * 16 + orow[i]) * 640 + ct2 * 80 + och[i] * 8) = o;
    }
  } else if (wave == 7) {
    PRIO(PW7);
    u64 ep;
    asm volatile("global_load_dwordx2 %0, %1, %2 sc1" : "=&v"(ep) : "v"(0u), "s"(p.ws + WS_EPOCH) : "memory");
    u32 dsk3 = gld2(p.dsk, (c0 + (lane < nch ? lane : 0)) * 2);
    w1_dma(w1b, smb, 2, 6, lane);
    __builtin_amdgcn_s_barrier();
    asm volatile("s_waitcnt vmcnt(6)" : "+v"(ep), "+v"(dsk3) :: "memory");
    {
      u64 t = ep * 0x9E3779B97F4A7C15ull + 0x632BE59BD9B4E019ull;
      tag = (u64)__builtin_amdgcn_readfirstlane((u32)t) | ((u64)__builtin_amdgcn_readfirstlane((u32)(t >> 32)) << 32);
      if (lane == 0) { lds_st(sm, SY_TAGLO, (u32)tag); lds_st(sm, SY_TAGHI, (u32)(tag >> 32)); LDRAIN(); lds_st(sm, SY_TAGOK, 1); }
    }
    w1_dma(w1b, smb, 8, 40, lane);
    STAMP(1);
    VMW(18);
    lds_post_nw(sm, SY_W1A, lane);
    DRAIN();
    lds_post_nw(sm, SY_W1, lane);
    STAMP(2);
    PRIO(3);
    lds_wait(sm, SY_LN, 2);
    {
      bf16x8 vt[10];
#pragma unroll
      for (int it = 0; it < 10; ++it) {
        LAS char* tp = sm + LD_X + (l15 >> 2) * LNS + (64 * it + 16 * g + 4 * (lane & 3)) * 2;
        vt[it] = cat(trd(tp), trd(tp + 4 * LNS));
      }
      lds_wait(sm, SY_FAOK, GATE);
#pragma unroll
      for (int it = 0; it < 10; ++it) BST(__builtin_bit_cast(u32x4, vt[it]), l16, WS_XNT + (lnb * 640 + 64 * it) * 16);
    }
    DRAIN();
    SET_FLAG(WS_FT + lnb * 64);
    STAMP(12);
    lds_wait(sm, SY_AISS, 4);
    polln<2>(p.ws, WS_FW, lane, 120, tag);
    lds_post_nw(sm, SY_W2RDY, lane);
    STAMP(13);
    polln<4>(p.ws, WS_FT, lane, 256, tag);
    if (b == 0 && lane == 0) __hip_atomic_store((GAS u64*)(p.ws + WS_EPOCH), ep + 1, RLX_AGENT);
    STAMP(3);
    {
      const int chl = lane & 3;
      u32x4 gv[16];
      const u32 go = ((lane >> 2) * 640 + c0 + chl) * 16 + (chl < nch ? 0u : OOB);
#pragma unroll
      for (int it = 0; it < 16; ++it) gv[it] = BLD(go, WS_XNT + it * 16 * 640 * 16);
      if (chl < 3) {
        LAS char* xd = sm + LD_X + chl * X_SZ;
        u32x4 z = {0, 0, 0, 0};
        *(LAS u32x4*)(xd + (lane >> 2) * 16) = z;
        *(LAS u32x4*)(xd + (14 + (lane >> 2)) * 16) = z;
#pragma unroll
        for (int it = 0; it < 16; ++it) *(LAS u32x4*)(xd + (30 + 16 * it + (lane >> 2)) * 16) = gv[it];
      }
    }
    lds_post(sm, SY_XRDY, lane);
    STAMP(4);
    PRIO(0);
    LOAD_BW();
    lds_wait(sm, SY_KRDY, nch);
    const int par = (l15 & 1) ^ 1;
    u32 aB = smb + LD_KT + (par ? KC1 : 0) + 2 * (2031 - l15 + 8 * g - par) - 3584;
    u32 bB = smb + LD_X + 16 * (28 + 2 * l15 + g);
    u32x4 fa[2][4], fb[2][4];
    { const CAddr c = caddr(aB, bB);
#pragma unroll
      for (int q = 0; q < 4; ++q) { fa[0][q] = FA_(c, q); fb[0][q] = FB_(c, q); } }
    lds_wait(sm, SY_MISH3, 1);
    PRIO(1);
    STAMP(5);
    u64 fc = 0;
#pragma unroll 1
    for (int c = 0; c < nch; ++c) {
      f32x4 y[4];
      const u32x4 dg = diag_frag(__builtin_amdgcn_readlane(dsk3, c), l15, g);
#pragma unroll
      for (int i = 0; i < 4; ++i) { f32x4 z = {0, 0, 0, 0}; y[i] = MFMA16(asb(dg), asb(fb[0][i]), z); }
      fc = ld_flag(p.ws, fcoff);
#pragma unroll 1
      for (int rho = 0; rho < 8; rho += 2) {
        if (c && rho == 4) { VMW(0); SET_FLAG(WS_FD + (c0 + c - 1) * 64); }
        conv_lo1(caddr(aB - 64, bB - 64), fa[0], fb[0], fa[1], fb[1], y);
        const bool last = rho == 6, more = c + 1 < nch;
        aB += last ? (more ? KT_SZ + 384 : 384) : -128; bB += last ? (more ? X_SZ + 384 : 384) : -128;
        conv_lo1(caddr(aB, bB), fa[1], fb[1], fa[0], fb[0], y);
      }
      store_y(rs, y, WS_YT + (c0 + c) * 4096, l15, g);
    }
    lds_post(sm, SY_CONV, lane);
    STAMP(6);
    if (!wall(fc == tag)) poll_flags(p.ws, fcoff, tag);
    CFENCE();
    LOAD_AF();
    VMW(18);
    SET_FLAG(WS_FD + (c0 + nch - 1) * 64);
    PRIO(0);
    STAMP(7);
    lds_wait(sm, SY_G1DONE, 4);
    gemm2<true>(rs, sm, wave, lane, rt2, nch, bw, af, res);
  } else {
    const int s = wave - 4;
    const bool hasch = s < nch;
    const int ch = hasch ? c0 + s : c0;
    LAS char* KT = sm + LD_KT + s * KT_SZ;
    u32 prm[6][3];
    const u32 po = ch * 384 + lane * 2;
    u32 ldt_raw = gld2(p.ldt, ch * 2), dsk_raw = gld2(p.dsk, ch * 2);
#pragma unroll
    for (int it = 0; it < 3; ++it) {
      prm[0][it] = gld2(p.Are, po + it * 128); prm[1][it] = gld2(p.Aim, po + it * 128); prm[2][it] = gld2(p.Bre, po + it * 128);
      prm[3][it] = gld2(p.Bim, po + it * 128); prm[4][it] = gld2(p.Cre, po + it * 128); prm[5][it] = gld2(p.Cim, po + it * 128);
    }
    __builtin_amdgcn_s_barrier();
    if (!hasch) {
      const int t2 = xcd * 16 + (j >> 1);
      PRIO(3);
      u32x4 wr[2][8];
      const u32 wo = g * 1280 + l15 * 16 + 3072;
#pragma unroll
      for (int pc = 0; pc < 2; ++pc) {
        const int pid = (t2 < 120 ? 2 * t2 : 0) + pc, rks = pid / 5, cb = pid - rks * 5;
        const char* src = (const char*)p.w2 + (32 * rks * 640 + 128 * cb) * 2 - 3072;
#pragma unroll
        for (int i = 0; i < 2; ++i) {
          setm0((u32)(size_t)KT + i * 4096);
          glds<0>(src + i * 20480, wo); glds<1024>(src + i * 20480 + 4096, wo); glds<2048>(src + i * 20480 + 8192, wo); glds<3072>(src + i * 20480 + 12288, wo);
        }
        DRAIN();
#pragma unroll
        for (int n = 0; n < 8; ++n) {
          LAS char* tp = KT + (16 * (g >> 1) + 4 * (g & 1) + (l15 >> 2)) * 256 + 32 * n + 8 * (lane & 3);
          wr[pc][n] = __builtin_bit_cast(u32x4, cat(trd(tp), trd(tp + 2048)));
        }
        LDRAIN();
      }
      LDRAIN(); STAMP(8);
      tag = get_tag(sm);
      lds_wait(sm, SY_FAOK, GATEW);
#pragma unroll
      for (int pc = 0; pc < 2; ++pc) {
        const int pid = 2 * t2 + pc, rks = pid / 5, cb = pid - rks * 5;
#pragma unroll
        for (int n = 0; n < 8; ++n) BST(wr[pc][n], l16 + n * 1024 + (t2 < 120 ? 0u : OOB), WS_W2F + (rks * 40 + 8 * cb) * 1024);
      }
      STAMP(9);
      DRAIN();
      STAMP(11);
      SET_FLAG(WS_FW + t2 * 64);
      PRIO(0);
    }
    asm volatile("s_waitcnt vmcnt(0)" : "+v"(ldt_raw), "+v"(dsk_raw) :: "memory");
#pragma unroll
    for (int k = 0; k < 6; ++k) asm volatile("" : "+v"(prm[k][0]), "+v"(prm[k][1]), "+v"(prm[k][2]));
    SCHED();
    STAMP(1);
    if (hasch) {
      const int m = l31;
      const float dt = rbf(__builtin_amdgcn_exp2f(bflo(ldt_raw) * 1.44269504089f));
      cf cc[3]; float mx = 0.f;
#pragma unroll
      for (int it = 0; it < 3; ++it) {
        const cf Bc = {bflo(prm[2][it]), bflo(prm[3][it])}, Cc = {bflo(prm[4][it]), bflo(prm[5][it])};
        cc[it] = cmul(Cc, Bc);
        mx = __builtin_fmaxf(mx, __builtin_fmaxf(__builtin_fabsf(cc[it].x), __builtin_fabsf(cc[it].y)));
      }
      const int ex = __builtin_amdgcn_frexp_expf(max64(mx));
      const float ksc = __builtin_amdgcn_ldexpf(dt, ex - 10);
      f32x16 kacc;
#pragma unroll
      for (int i = 0; i < 16; ++i) kacc[i] = 0.f;
      const int baseA = 512 * h + 16 * ((m >> 2) ^ (h << 1)) + 4 * (m & 3);
      const int baseB = 4096 + 2048 * h + 16 * (m >> 2) + 4 * (m & 3);
      const int rdA = m * 128, rsw = (m >> 1) & 7;
      u32x4 ta[4], tb[4];
#define KMFMA(ks) kacc = MFMA32H(__builtin_bit_cast(f16x8, ta[ks]), __builtin_bit_cast(f16x8, tb[ks]), kacc)
#define WRA(rr, v) (*(LAS u32*)(KT + ((baseA ^ (16 * ((((rr) & 3) >> 1) | ((((rr) >> 2) & 1) << 2)))) + 128 * (((rr) & 3) + 8 * ((rr) >> 2)))) = (v))
#define WRB(be, v) (*(LAS u32*)(KT + ((baseB ^ (16 * ((be) >> 1))) + 128 * (be))) = (v))
#define SWAP(dst, a0, a1) do { auto q_ = __builtin_amdgcn_permlane32_swap((a0), (a1), false, false); dst[0] = q_[0]; dst[1] = q_[1]; } while (0)
#pragma unroll
      for (int it = 0; it < 3; ++it) {
        const float lr = dt * bflo(prm[0][it]) * 1.44269504089f, ph = dt * bflo(prm[1][it]) * 0.15915494309f;
        const cf c = {__builtin_amdgcn_ldexpf(cc[it].x, 10 - ex), __builtin_amdgcn_ldexpf(cc[it].y, 10 - ex)};
        const cf e1 = cexp2r(lr, ph), e128 = cexp2r(lr * 128.f, ph * 128.f);
        const cf e2 = csq(e1), e256 = csq(e128), e4 = csq(e2), e512 = csq(e256);
        SCHED(); if (it) KMFMA(0); SCHED();
        const cf e8 = csq(e4), e1024 = csq(e512), e16 = csq(e8), e32 = csq(e16);
        SCHED(); if (it) KMFMA(1); SCHED();
        const cf e64 = csq(e32), c512 = cmul(c, e512), c1024 = cmul(c, e1024);
        const u32 g1 = toh(e1), g2 = toh(e2), g4 = toh(e4), g8 = toh(e8), g16 = toh(e16);
        SCHED(); if (it) KMFMA(2); SCHED();
        const u32 g256 = toh(e256), g512 = toh(e512), k32 = tohc(e32), k64 = tohc(e64), k128 = tohc(e128);
        const u32 g3 = hmul(g2, g1), g24 = hmul(g16, g8), k96 = hmul(k64, k32);
        const u32 b0 = toh(c), b7 = toh(c512), b10 = toh(c1024);
        SCHED(); if (it) KMFMA(3); SCHED();
        u32 R0[2], R7[2], R10[2], P[4][2], H1[2], H2[2], H3[2], H4[2], H8[2], H16[2], H24[2], H256[2];
        SWAP(R0, b0, hmul(b0, g4)); SWAP(R7, b7, hmul(b7, g256)); SWAP(R10, b10, hmul(b10, g512));
        SWAP(P[0], 0x00003C00u, k128); SWAP(P[1], k32, hmul(k128, k32)); SWAP(P[2], k64, hmul(k128, k64)); SWAP(P[3], k96, hmul(k128, k96));
        SWAP(H1, g1, g1); SWAP(H2, g2, g2); SWAP(H3, g3, g3); SWAP(H4, g4, g4); SWAP(H8, g8, g8); SWAP(H16, g16, g16); SWAP(H24, g24, g24); SWAP(H256, g256, g256);
        SCHED();
#pragma unroll
        for (int sp = 0; sp < 2; ++sp) {
#pragma unroll
          for (int a = 0; a < 4; ++a) {
            WRB(4 * a, P[a][sp]); WRB(4 * a + 1, hmulc(P[a][sp], H8[sp])); SCHED();
            WRB(4 * a + 2, hmulc(P[a][sp], H16[sp])); WRB(4 * a + 3, hmulc(P[a][sp], H24[sp])); SCHED();
            if (sp) { KMFMA(a); SCHED(); }
          }
          const u32 r4 = hmul(R0[sp], H256[sp]), r12 = hmul(R10[sp], H256[sp]);
          WRA(0, R0[sp]); WRA(1, hmul(R0[sp], H1[sp])); SCHED();
          WRA(2, hmul(R0[sp], H2[sp])); WRA(3, hmul(R0[sp], H3[sp])); SCHED();
          WRA(4, r4); WRA(5, hmul(r4, H2[sp])); SCHED();
          WRA(6, hmul(r4, H4[sp])); WRA(7, R7[sp]); SCHED();
          WRA(8, hmul(R7[sp], H4[sp])); WRA(9, hmul(R7[sp], H8[sp])); SCHED();
          WRA(10, R10[sp]); WRA(11, hmul(R10[sp], H8[sp])); SCHED();
          WRA(12, r12); WRA(13, hmul(r12, H8[sp]));
          CFENCE(); SCHED();
#pragma unroll
          for (int ks = 0; ks < 4; ++ks) {
            const int so = rdA + 16 * ((2 * ks + h) ^ rsw);
            ta[ks] = *(LAS u32x4*)(KT + so); tb[ks] = *(LAS u32x4*)(KT + 4096 + so);
          }
          CFENCE(); SCHED();
        }
      }
      KMFMA(0); KMFMA(1); KMFMA(2); KMFMA(3);
      CFENCE();
      STAMP(2);
      {
        const int bb = m;
        float kv[14];
#pragma unroll
        for (int i = 0; i < 14; ++i) kv[i] = kacc[i] * ksc;
        u32x2 s0 = {pk2(kv[3], kv[2]), pk2(kv[1], kv[0])};
        *(LAS u32x2*)(KT + 2 * (2044 - 8 * bb - 4 * h)) = s0;
        u32x2 s1 = {pk2(kv[6], kv[5]), pk2(kv[4], kv[4])};
        *(LAS u32x2*)(KT + 2 * (2044 - 256 - 8 * bb - 4 * h)) = s1;
        u32x4 s2 = {pk2(kv[9], kv[9]), pk2(kv[8], kv[8]), pk2(kv[8], kv[7]), pk2(kv[7], kv[7])};
        *(LAS u32x4*)(KT + 2 * (2040 - 512 - 256 * h - 8 * bb)) = s2;
#pragma unroll
        for (int a2 = 0; a2 < 2; ++a2) {
          const float v0 = kv[10 + 2 * a2], v8 = kv[11 + 2 * a2], tie = (bb & 1) ? v8 : v0;
          u32x4 s3 = {pk2(v8, v8), pk2(v8, tie), pk2(v0, v0), pk2(v0, v0)};
          *(LAS u32x4*)(KT + 2 * (2040 - 1024 - 512 * h - 256 * a2 - 8 * bb)) = s3;
        }
        u32x4 z = {0, 0, 0, 0};
        if (lane < 4) *(LAS u32x4*)(KT + 4096 + (lane & 1) * 16 + (lane >> 1) * KC1) = z;
        CFENCE();
        u32 dw[17];
#pragma unroll
        for (int k = 0; k < 4; ++k) { u32x4 v = *(LAS u32x4*)(KT + lane * 64 + k * 16); dw[4 * k] = v.x; dw[4 * k + 1] = v.y; dw[4 * k + 2] = v.z; dw[4 * k + 3] = v.w; }
        dw[16] = *(LAS u32*)(KT + lane * 64 + 64);
#pragma unroll
        for (int k = 0; k < 4; ++k) {
          u32x4 v;
#pragma unroll
          for (int e = 0; e < 4; ++e) v[e] = __builtin_amdgcn_alignbit(dw[4 * k + e + 1], dw[4 * k + e], 16);
          *(LAS u32x4*)(KT + KC1 + lane * 64 + k * 16) = v;
        }
      }
      lds_post(sm, SY_KRDY, lane);
    }
    STAMP(3);
    tag = get_tag(sm);
    lds_wait(sm, SY_W2RDY, 1);
    LOAD_BW();
    if (hasch) {
      f32x4 y[4];
      const int par = (l15 & 1) ^ 1;
      u32 aB = (u32)(size_t)KT + (par ? KC1 : 0) + 2 * (2031 - l15 + 8 * g - par) - 3584;
      u32 bB = smb + LD_X + s * X_SZ + 16 * (28 + 2 * l15 + g);
      u32x4 fa[8], fb[8];
      lds_wait(sm, SY_XRDY, 1);
      { const CAddr c = caddr(aB, bB);
#pragma unroll
        for (int q = 0; q < 8; ++q) { fb[7 - q] = FB_(c, 7 - q); fa[q] = FA_(c, q); } }
      const u32x4 dg = diag_frag(dsk_raw, l15, g);
      SCHED();
      lds_wait(sm, SY_MISH0 + s, 1);
      PRIO(1);
      STAMP(4);
#pragma unroll
      for (int i = 0; i < 4; ++i) { f32x4 z = {0, 0, 0, 0}; y[i] = MFMA16(asb(dg), asb(fb[4 + i]), z); }
      u64 fc = 0;
#pragma unroll 1
      for (int rho = 0; rho < 8; rho += 2) {
        fc = ld_flag(p.ws, fcoff);
        const u32 nx = rho < 6 ? 128 : 0;
        conv_hi2(caddr(aB - 64, bB - 64), caddr(aB - nx, bB - nx), fa, fb, y); aB -= 128; bB -= 128;
      }
      STAMP(5);
      store_y(rs, y, WS_YT + ch * 4096 + 2048, l15, g);
      lds_post(sm, SY_CONV, lane);
      STAMP(6);
      if (!wall(fc == tag)) poll_flags(p.ws, fcoff, tag);
      CFENCE();
      LOAD_AF();
      VMW(18);
      SET_FLAG(WS_FD + (640 + ch) * 64);
      PRIO(0);
      STAMP(7);
    } else {
      lds_wait(sm, SY_MISH0 + s, 1);
      poll_flags(p.ws, fcoff, tag);
      LOAD_AF();
    }
    lds_wait(sm, SY_G1DONE, 4);
    gemm2<true>(rs, sm, wave, lane, rt2, nch, bw, af, res);
  }
  STAMP(10);
}

extern "C" void kernel_launch(void* const* d_in, const int* in_sizes, int n_in, void* d_out, int out_size,
                              void* d_ws, size_t ws_size, hipStream_t stream) {
  Params p;
  p.x = (const u16*)d_in[0]; p.Are = (const u16*)d_in[1]; p.Aim = (const u16*)d_in[2]; p.Bre = (const u16*)d_in[3];
  p.Bim = (const u16*)d_in[4]; p.Cre = (const u16*)d_in[5]; p.Cim = (const u16*)d_in[6]; p.ldt = (const u16*)d_in[7];
  p.dsk = (const u16*)d_in[8]; p.w1 = (const u16*)d_in[10]; p.b1 = (const u16*)d_in[11]; p.w2 = (const u16*)d_in[12];
  p.out = (u16*)d_out; p.ws = (char*)d_ws;
  (void)hipFuncSetAttribute((const void*)fused, hipFuncAttributeMaxDynamicSharedMemorySize, LDS_BYTES);
  fused<<<256, 512, LDS_BYTES, stream>>>(p);
}
```

```cpp
#include <hip/hip_runtime.h>
#include <stdint.h>
typedef unsigned short u16; typedef unsigned u32; typedef unsigned long long u64;
typedef __attribute__((ext_vector_type(2))) float f32x2;
typedef __attribute__((ext_vector_type(4))) float f32x4;
typedef __attribute__((ext_vector_type(16))) float f32x16;
typedef __attribute__((ext_vector_type(2))) __bf16 bf16x2;
typedef __attribute__((ext_vector_type(4))) __bf16 bf16x4;
typedef __attribute__((ext_vector_type(8))) __bf16 bf16x8;
typedef __attribute__((ext_vector_type(2))) _Float16 f16x2;
typedef __attribute__((ext_vector_type(8))) _Float16 f16x8;
typedef __attribute__((ext_vector_type(2))) u32 u32x2;
typedef __attribute__((ext_vector_type(3))) u32 u32x3;
typedef __attribute__((ext_vector_type(4))) u32 u32x4;
#define LAS __attribute__((address_space(3)))
#define GAS __attribute__((address_space(1)))
#define RLX_AGENT __ATOMIC_RELAXED, __HIP_MEMORY_SCOPE_AGENT
#define RLX_WG __ATOMIC_RELAXED, __HIP_MEMORY_SCOPE_WORKGROUP
#if defined(__HIP_DEVICE_COMPILE__)
#define NOPK __attribute__((target("no-packed-fp32-ops")))
#else
#define NOPK
#endif
#define DEV __device__ __forceinline__ NOPK
#define CFENCE() asm volatile("" ::: "memory")
#define DRAIN() asm volatile("s_waitcnt vmcnt(0)" ::: "memory")
#define LDRAIN() asm volatile("s_waitcnt lgkmcnt(0)" ::: "memory")
#define VMW(n) asm volatile("s_waitcnt vmcnt(%0)" :: "i"(n) : "memory")
#define SCHED() __builtin_amdgcn_sched_barrier(0)
#define PRIO(n) __builtin_amdgcn_s_setprio(n)
#define DMA_G 14
#define BWS 1
#define G2MAP 0
#define GATE 0
#define GATEW 0
#define PW7 1
#define PSLEEP 4

constexpr int L_ = 2048, D_ = 640, N_ = 192, F_ = 1536;
constexpr u32 WS_EPOCH = 0;
constexpr u32 WS_FT = 4096;
constexpr u32 WS_FW = WS_FT + 256 * 64;
constexpr u32 WS_FD = WS_FW + 128 * 64;
constexpr u32 WS_FC = WS_FD + 1280 * 64;
constexpr u32 WS_FA = WS_FC + 1024 * 64;
constexpr u32 WS_XNA = WS_FA + 512 * 64;
constexpr u32 WS_XNT = WS_XNA + L_ * D_ * 2;
constexpr u32 WS_W2F = WS_XNT + L_ * D_ * 2;
constexpr u32 WS_ACT = WS_W2F + F_ * D_ * 2;
constexpr u32 WS_YT = WS_ACT + L_ * F_ * 2;
constexpr u32 WS_END = WS_YT + L_ * D_ * 2;
constexpr u32 WS_STAMP = WS_END;
constexpr u32 OOB = 0x40000000u;
constexpr int KT_SZ = 8320, KC1 = 4160, X_SZ = 4608;
constexpr int LD_KT = 122880, LD_X = LD_KT + 3 * KT_SZ;
constexpr int LD_SYNC = LD_X + 3 * X_SZ;
constexpr int LDS_BYTES = 163840;
constexpr int LD_SP = 61440;
constexpr int LNS = 1296;
enum { SY_W1 = 0, SY_G1DONE, SY_W2RDY, SY_TAGLO, SY_TAGHI, SY_TAGOK, SY_KRDY, SY_CONV, SY_MISH0, SY_MISH1, SY_MISH2, SY_MISH3,
       SY_XRDY, SY_LN, SY_FAOK, SY_AISS, SY_GPART, SY_YST, SY_SP0, SY_SP1, SY_SP2, SY_SP3, SY_W1A };
constexpr u32 SPIN_CAP = 1u << 18;

struct Params {
  const u16 *x, *Are, *Aim, *Bre, *Bim, *Cre, *Cim, *ldt, *dsk, *w1, *b1, *w2;
  u16* out; char* ws;
};

DEV float asf(u32 v) { return __builtin_bit_cast(float, v); }
DEV u32 asu(float v) { return __builtin_bit_cast(u32, v); }
DEV float bflo(u32 v) { return asf(v << 16); }
DEV float bfhi(u32 v) { return asf(v & 0xffff0000u); }
DEV u32 pk2(float a, float b) { f32x2 v = {a, b}; return __builtin_bit_cast(u32, __builtin_convertvector(v, bf16x2)); }
DEV float rbf(float a) { return bflo(pk2(a, a)); }
struct cf { float x, y; };
DEV cf cmul(cf a, cf b) { cf r = {__builtin_fmaf(-a.y, b.y, a.x * b.x), __builtin_fmaf(a.y, b.x, a.x * b.y)}; return r; }
DEV cf cmulc(cf a, cf b) { cf r = {__builtin_fmaf(a.y, b.y, a.x * b.x), __builtin_fmaf(a.y, b.x, -(a.x * b.y))}; return r; }
DEV cf csq(cf a) { cf r = {__builtin_fmaf(-a.y, a.y, a.x * a.x), (a.x + a.x) * a.y}; return r; }
DEV cf cexp2r(float lr, float ph) {
  float e = __builtin_amdgcn_exp2f(lr); float f = __builtin_amdgcn_fractf(ph);
  cf r = {e * __builtin_amdgcn_cosf(f), e * __builtin_amdgcn_sinf(f)}; return r; }
DEV cf csel(bool h, cf a) { cf r = {h ? a.x : 1.f, h ? a.y : 0.f}; return r; }
DEV u32 toh(cf a) { f32x2 v = {a.x, a.y}; return __builtin_bit_cast(u32, __builtin_convertvector(v, f16x2)); }
DEV u32 tohc(cf a) { f32x2 v = {a.x, -a.y}; return __builtin_bit_cast(u32, __builtin_convertvector(v, f16x2)); }
DEV u32 hmul(u32 a_, u32 b_) {
  const f16x2 a = __builtin_bit_cast(f16x2, a_), b = __builtin_bit_cast(f16x2, b_), nb = -b;
  const f16x2 s = __builtin_shufflevector(nb, b, 1, 2);
  return __builtin_bit_cast(u32, __builtin_elementwise_fma(a.yy, s, a.xx * b)); }
DEV u32 hmulc(u32 a_, u32 b_) {
  const f16x2 a = __builtin_bit_cast(f16x2, a_), b = __builtin_bit_cast(f16x2, b_), nb = -b;
  const f16x2 s = __builtin_shufflevector(b, nb, 0, 3);
  return __builtin_bit_cast(u32, __builtin_elementwise_fma(a.yy, b.yx, a.xx * s)); }
template <int CTRL> DEV float dppf(float v) { return asf(__builtin_amdgcn_update_dpp(0, asu(v), CTRL, 0xf, 0xf, true)); }
DEV float red8(float v) { v += dppf<0xB1>(v); v += dppf<0x4E>(v); v += dppf<0x141>(v); return v; }
DEV float red_mod4(float v) {
  v += dppf<0x124>(v); v += dppf<0x128>(v);
  auto r = __builtin_amdgcn_permlane16_swap(asu(v), asu(v), false, false);
  v = asf(r[0]) + asf(r[1]);
  auto q = __builtin_amdgcn_permlane32_swap(asu(v), asu(v), false, false);
  return asf(q[0]) + asf(q[1]); }
DEV float max64(float v) {
  v = __builtin_fmaxf(v, dppf<0xB1>(v)); v = __builtin_fmaxf(v, dppf<0x4E>(v)); v = __builtin_fmaxf(v, dppf<0x141>(v)); v = __builtin_fmaxf(v, dppf<0x140>(v));
  auto r = __builtin_amdgcn_permlane16_swap(asu(v), asu(v), false, false);
  v = __builtin_fmaxf(asf(r[0]), asf(r[1]));
  auto q = __builtin_amdgcn_permlane32_swap(asu(v), asu(v), false, false);
  return __builtin_fmaxf(asf(q[0]), asf(q[1])); }
DEV bool wall(bool c) { return __builtin_amdgcn_ballot_w64(c) == ~0ull; }

DEV u32 lds_ld(LAS char* s, int w) { return __hip_atomic_load((LAS u32*)(s + LD_SYNC) + w, RLX_WG); }
DEV void lds_st(LAS char* s, int w, u32 v) { __hip_atomic_store((LAS u32*)(s + LD_SYNC) + w, v, RLX_WG); }
DEV void lds_wait(LAS char* s, int w, u32 want) {
#pragma unroll 1
  for (u32 n = 0; n < (1u << 22); ++n) { if (__builtin_amdgcn_readfirstlane(lds_ld(s, w)) >= want) break; __builtin_amdgcn_s_sleep(1); }
  CFENCE();
}
DEV void lds_post(LAS char* s, int w, int lane) {
  LDRAIN();
  if (lane == 0) (void)__hip_atomic_fetch_add((LAS u32*)(s + LD_SYNC) + w, 1u, RLX_WG);
  CFENCE();
}
DEV void lds_post_nw(LAS char* s, int w, int lane) {
  CFENCE();
  if (lane == 0) (void)__hip_atomic_fetch_add((LAS u32*)(s + LD_SYNC) + w, 1u, RLX_WG);
  CFENCE();
}
DEV u64 get_tag(LAS char* s) {
  lds_wait(s, SY_TAGOK, 1);
  return (u64)__builtin_amdgcn_readfirstlane(lds_ld(s, SY_TAGLO)) | ((u64)__builtin_amdgcn_readfirstlane(lds_ld(s, SY_TAGHI)) << 32);
}
DEV u64 ld_flag(char* ws, u32 off) { return __hip_atomic_load((GAS u64*)(ws + off), RLX_AGENT); }
DEV void poll_flags(char* ws, u32 off, u64 tag) {
#pragma unroll 1
  for (u32 n = 0; n < SPIN_CAP; ++n) {
    if (wall(ld_flag(ws, off) == tag)) break;
    __builtin_amdgcn_s_sleep(PSLEEP);
  }
  CFENCE();
}
template <int NL> DEV void polln(char* ws, u32 base, int lane, int nvalid, u64 tag) {
#pragma unroll 1
  for (u32 n = 0; n < SPIN_CAP; ++n) {
    bool ok = true;
#pragma unroll
    for (int k = 0; k < NL; ++k) { int i = lane + 64 * k; u64 v = ld_flag(ws, base + i * 64); ok &= (v == tag) | (i >= nvalid); }
    if (wall(ok)) break;
    __builtin_amdgcn_s_sleep(16);
  }
  CFENCE();
}
DEV bf16x4 trd(LAS char* p) { return __builtin_amdgcn_ds_read_tr16_b64_v4bf16((LAS bf16x4*)p); }
DEV bf16x8 cat(bf16x4 a, bf16x4 b) { return __builtin_shufflevector(a, b, 0, 1, 2, 3, 4, 5, 6, 7); }
DEV bf16x8 asb(u32x4 v) { return __builtin_bit_cast(bf16x8, v); }
DEV LAS char* lp(u32 a) { return (LAS char*)(size_t)a; }
#define MFMA16(a, b, c) __builtin_amdgcn_mfma_f32_16x16x32_bf16(a, b, c, 0, 0, 0)
#define MFMA32(a, b, c) __builtin_amdgcn_mfma_f32_32x32x16_bf16(a, b, c, 0, 0, 0)
#define MFMA32H(a, b, c) __builtin_amdgcn_mfma_f32_32x32x16_f16(a, b, c, 0, 0, 0)
#define BLD(voff, soff) __builtin_amdgcn_raw_buffer_load_b128(rs, (voff), (soff), 16)
#define BST(v, voff, soff) __builtin_amdgcn_raw_buffer_store_b128((v), rs, (voff) + (soff), 0, 16)

#define SET_FLAG(off) do { const u32x4 f_ = {(u32)tag, (u32)(tag >> 32), (u32)tag, (u32)(tag >> 32)}; BST(f_, l16 + (lane < 4 ? 0u : OOB), (off)); } while (0)

#define STAMP(i)

DEV void setm0(u32 v) { asm volatile("s_mov_b32 m0, %0\n\ts_nop 0" :: "s"(v) : "memory"); }
template <int OFF> DEV void glds(const void* sbase, u32 voff) {
  asm volatile("global_load_lds_dwordx4 %0, %1 offset:%2" :: "v"(voff), "s"(sbase), "i"(OFF) : "memory");
}
DEV void w1_dma(const char* wb, u32 smb, int first, int last, int lane) {
  u32 so[3];
#pragma unroll
  for (int r = 0; r < 3; ++r) { const int c = r * 64 + lane, row = c / 12, cc = c - row * 12; so[r] = row * 3072 + cc * 16 + 2048 - 1024 * r; }
#pragma unroll 1
  for (int i = first; i < last; i += 3) {
    const char* src = wb + i * 16 * 3072 - 2048;
    setm0(smb + i * 3072);
    glds<0>(src, so[0]); glds<1024>(src, so[1]); glds<2048>(src, so[2]);
  }
}
DEV u32x4 ldA(u32 a) { LAS u32* ap = (LAS u32*)lp(a); u32x4 v = {ap[0], ap[1], ap[2], ap[3]}; return v; }
DEV u32x4 ldB(u32 a) { return *(LAS u32x4*)lp(a); }
struct CAddr { u32 a[4]; u32 b; };
DEV CAddr caddr(u32 aB, u32 bB) {
  CAddr c;
#pragma unroll
  for (int i = 0; i < 4; ++i) { c.a[i] = aB + 1024 * (3 - i); asm("" : "+v"(c.a[i])); }
  c.b = bB; asm("" : "+v"(c.b));
  return c;
}
#define FA_(c, q) ldA((c).a[(q) >> 1] + (((q) & 1) ? 0 : 512))
#define FB_(c, q) ldB((c).b + 512 * (q))
DEV void conv_hi2(const CAddr& n1, const CAddr& n2, u32x4 (&fa)[8], u32x4 (&fb)[8], f32x4 (&y)[4]) {
#pragma unroll
  for (int q = 0; q < 8; ++q) {
#pragma unroll
    for (int pp = 7 - q; pp >= 0 && pp + q >= 4; --pp) {
      y[q + pp - 4] = MFMA16(asb(fa[q]), asb(fb[pp]), y[q + pp - 4]);
      if (pp == 7 - q) { SCHED(); fb[pp] = FB_(n1, pp); SCHED(); }
    }
    SCHED(); fa[q] = FA_(n1, q); SCHED();
  }
#pragma unroll
  for (int pp = 7; pp >= 0; --pp) {
#pragma unroll
    for (int q = (pp < 4 ? 4 - pp : 0); q <= 7 - pp; ++q) {
      y[q + pp - 4] = MFMA16(asb(fa[q]), asb(fb[pp]), y[q + pp - 4]);
      if (pp == (q < 4 ? 4 - q : 0)) { SCHED(); fa[q] = FA_(n2, q); SCHED(); }
    }
    SCHED(); fb[pp] = FB_(n2, pp); SCHED();
  }
}
DEV void conv_lo1(const CAddr& n, const u32x4 (&ca)[4], const u32x4 (&cb)[4], u32x4 (&na)[4], u32x4 (&nb)[4], f32x4 (&y)[4]) {
  int k = 0;
#pragma unroll
  for (int q = 0; q < 4; ++q)
#pragma unroll
    for (int pp = 3 - q; pp >= 0; --pp) {
      y[q + pp] = MFMA16(asb(ca[q]), asb(cb[pp]), y[q + pp]);
      SCHED();
      if (k < 8) { if (k & 1) nb[3 - (k >> 1)] = FB_(n, 3 - (k >> 1)); else na[k >> 1] = FA_(n, k >> 1); }
      ++k;
      SCHED();
    }
}
DEV u32x4 diag_frag(u32 dsk, int l15, int g) {
  const u32 v = (g == 2 + (l15 >> 3)) ? ((l15 & 1) ? dsk << 16 : dsk) : 0u;
  const int w = (l15 & 7) >> 1;
  u32x4 r = {w == 0 ? v : 0u, w == 1 ? v : 0u, w == 2 ? v : 0u, w == 3 ? v : 0u};
  return r;
}
DEV void store_y(const __amdgpu_buffer_rsrc_t rs, const f32x4 (&y)[4], u32 base, int l15, int g) {
#pragma unroll
  for (int t = 0; t < 4; t += 2) {
    const u32 a0 = pk2(y[t].x, y[t].y), a1 = pk2(y[t].z, y[t].w), b0 = pk2(y[t + 1].x, y[t + 1].y), b1 = pk2(y[t + 1].z, y[t + 1].w);
    auto s0 = __builtin_amdgcn_permlane16_swap(a0, b0, false, false);
    auto s1 = __builtin_amdgcn_permlane16_swap(a1, b1, false, false);
    const u32x4 o = {s0[0], s1[0], s0[1], s1[1]};
    BST(o, (t + (g & 1)) * 512 + (l15 >> 2) * 128 + (g >> 1) * 64 + (l15 & 3) * 16, base);
  }
}
DEV u32 gld2(const void* sbase, u32 voff) { u32 r; asm volatile("global_load_ushort %0, %1, %2" : "=&v"(r) : "v"(voff), "s"(sbase) : "memory"); return r; }

template <bool SW>
DEV void gemm2(const __amdgpu_buffer_rsrc_t rs, LAS char* sm, int wave, int lane, int rt2, int nch,
               u32x4 (&bw)[6][5], u32x4 (&af)[4][6], f32x4 (&res)[5]) {
  const int own = wave & 3;
  const u32 l16 = lane * 16;
  LAS char* dst = sm + wave * 15360 + l16;
  f32x4 acc[2][5];
#pragma unroll
  for (int st = 0; st < 4; ++st) {
#pragma unroll
    for (int k = 0; k < 6; ++k) {
#pragma unroll
      for (int n = 0; n < 5; ++n) {
        f32x4 z = {0, 0, 0, 0};
        acc[st & 1][n] = MFMA16(asb(bw[k][n]), asb(af[st][k]), k ? acc[st & 1][n] : z);
      }
      SCHED();
      if (st == 0) af[3][k] = BLD(l16 + (k & 3) * 1024, WS_ACT + ((rt2 * 4 + own) * 48 + wave * 6 + (k & 4)) * 1024);
      if (st > 0 && k < 5) *(LAS f32x4*)(dst + ((st - 1) * 5 + k) * 1024) = acc[(st - 1) & 1][k];
      if (st > 0 && k == 5) { if (SW) lds_post_nw(sm, SY_SP0 + own, lane); else if (st == 3) lds_post_nw(sm, SY_GPART, lane); }
      SCHED();
    }
  }
  if (SW) {
    lds_wait(sm, SY_CONV, nch + 1);
#pragma unroll
    for (int n = 0; n < 5; ++n) *(LAS f32x4*)(sm + LD_KT + own * 5120 + n * 1024 + l16) = acc[1][n];
    lds_post_nw(sm, SY_SP0 + own, lane);
  } else {
#pragma unroll
    for (int n = 0; n < 5; ++n) res[n] = acc[1][n];
  }
}

__global__ __launch_bounds__(512, 2) NOPK void fused(Params p) {
  extern __shared__ __attribute__((aligned(16))) char smem_[];
  LAS char* sm = (LAS char*)smem_;
  const u32 smb = (u32)(size_t)sm;
  const int b = __builtin_amdgcn_workgroup_id_x(), xcd = b & 7, j = b >> 3;
  const int tid = __builtin_amdgcn_workitem_id_x(), wave = __builtin_amdgcn_readfirstlane(tid >> 6), lane = tid & 63;
  const int g = lane >> 4, l15 = lane & 15, l31 = lane & 31, h = lane >> 5;
  const u32 l16 = lane * 16;
  const __amdgpu_buffer_rsrc_t rs = __builtin_amdgcn_make_buffer_rsrc(p.ws, 0, WS_END, 0x00020000);
  STAMP(0);
  const int rt2 = xcd * 4 + (j & 3), ct2 = j >> 2;
  const int rp = (xcd & 3) * 4 + (j & 3), cs = (xcd >> 2) * 8 + (j >> 2);
  const int lnb = xcd * 32 + j;
  const int c0 = xcd * 80 + (j >> 1) * 5 + ((j & 1) ? 3 : 0), nch = (j & 1) ? 2 : 3;
  const int own = wave & 3;
  const u32 fcoff = WS_FC + ((rt2 * 8 + wave) * 4 + (lane & 3)) * 64;
  const char* w1b = (const char*)p.w1 + cs * 192;
  u64 tag;
  f32x4 res[5];
  u32x4 bw[6][5], af[4][6];
#define LOAD_BW1(q) bw[(q) / 5][(q) % 5] = BLD(l16 + (((q) % 5) & 3) * 1024, WS_W2F + ((wave * 6 + (q) / 5) * 40 + ct2 * 5 + (((q) % 5) & 4)) * 1024)
#define LOAD_BW() do { _Pragma("unroll") for (int q = 0; q < 30; ++q) LOAD_BW1(q); SCHED(); } while (0)
#define LOAD_AF() do { _Pragma("unroll") for (int st = 0; st < 3; ++st) _Pragma("unroll") for (int k = 0; k < 6; ++k) \
    af[st][k] = BLD(l16 + (k & 3) * 1024, WS_ACT + ((rt2 * 4 + ((own + 1 + st) & 3)) * 48 + wave * 6 + (k & 4)) * 1024); SCHED(); } while (0)

  if (wave < 4) {
    const int gw = wave;
    u32 braw[3];
    if (gw < 2) {
      PRIO(3);
      const int row = lane & 3, c16 = lane >> 2;
      u32x4 xv[5];
#pragma unroll
      for (int i = 0; i < 5; ++i) xv[i] = *(const u32x4*)(p.x + (lnb * 8 + gw * 4 + row) * 640 + (16 * i + c16) * 8);
      if (tid < 32) lds_st(sm, tid, 0);
      LDRAIN();
      __builtin_amdgcn_s_barrier();
      SCHED();
      const bf16x2 ones = __builtin_bit_cast(bf16x2, 0x3F803F80u);
      float s2[2] = {0.f, 0.f}, q2[2] = {0.f, 0.f};
#pragma unroll
      for (int i = 0; i < 5; ++i)
#pragma unroll
        for (int k = 0; k < 4; ++k) {
          const u32 xw = xv[i][k]; const bf16x2 v = __builtin_bit_cast(bf16x2, xw);
          s2[k & 1] = __builtin_amdgcn_fdot2_f32_bf16(v, ones, s2[k & 1], false);
          q2[k & 1] = __builtin_amdgcn_fdot2_f32_bf16(v, v, q2[k & 1], false);
        }
      const float mean = red_mod4(s2[0] + s2[1]) * (1.f / 640.f);
      const float var = __builtin_fmaxf(__builtin_fmaf(-mean, mean, red_mod4(q2[0] + q2[1]) * (1.f / 640.f)), 0.f);
      const float sd = rbf(__builtin_amdgcn_sqrtf(rbf(rbf(var) + 1e-5f)));
      const float rinv = __builtin_amdgcn_rcpf(sd), nmr = -rbf(mean) * rinv;
      LAS char* sc = sm + LD_X;
#pragma unroll
      for (int i = 0; i < 5; ++i) {
        u32x4 o;
#pragma unroll
        for (int k = 0; k < 4; ++k) o[k] = pk2(__builtin_fmaf(bflo(xv[i][k]), rinv, nmr), __builtin_fmaf(bfhi(xv[i][k]), rinv, nmr));
        BST(o, l16 + i * 1024, WS_XNA + (lnb * 2 + gw) * 5120);
        xv[i] = o;
      }
      STAMP(1);
      tag = get_tag(sm);
      DRAIN();
      SET_FLAG(WS_FA + (lnb * 2 + gw) * 64);
      STAMP(2);
#pragma unroll
      for (int i = 0; i < 5; ++i) *(LAS u32x4*)(sc + (gw * 4 + row) * LNS + (16 * i + c16) * 16) = xv[i];
      lds_post_nw(sm, SY_LN, lane);
#pragma unroll
      for (int n = 0; n < 3; ++n) braw[n] = p.b1[cs * 96 + n * 32 + l31];
    } else {
      w1_dma(w1b, smb, gw - 2, gw - 1, lane);
      __builtin_amdgcn_s_barrier();
      w1_dma(w1b, smb, gw + 1, 40, lane);
      STAMP(1);
      VMW(18);
      lds_post_nw(sm, SY_W1A, lane);
#pragma unroll
      for (int n = 0; n < 3; ++n) braw[n] = p.b1[cs * 96 + n * 32 + l31];
      DRAIN();
      lds_post_nw(sm, SY_W1, lane);
      STAMP(2);
      tag = get_tag(sm);
    }
    poll_flags(p.ws, WS_FA + (rp * 32 + gw * 8 + (lane & 7)) * 64, tag);
    lds_post_nw(sm, SY_FAOK, lane);
    PRIO(0);
    STAMP(3);
    u32x4 a[40];
    const u32 av = (l31 >> 2) * 5120 + h * 64 + (l31 & 3) * 16;
#define LOAD_A(t) a[t] = BLD(av + ((t) & 31) * 128, WS_XNA + (rp * 32 + gw * 8) * 5120 + ((t) >> 5) * 4096)
#pragma unroll
    for (int t = 0; t < 32; ++t) LOAD_A(t);
    SCHED();
    lds_wait(sm, SY_W1A, 3);
    PRIO(2);
    f32x16 acc[3];
    {
      u32 wb[2];
      wb[0] = smb + 192 * (8 * h + (l15 >> 2)) + 32 * (g & 1) + 8 * (lane & 3); wb[1] = wb[0] + 20 * 3072;
      asm("" : "+v"(wb[0]), "+v"(wb[1]));
      bf16x8 wf[3][3];
#define RDW(t, n) wf[(t) % 3][n] = cat(trd(lp(wb[(t) / 20] + ((t) % 20) * 3072 + (n) * 64)), trd(lp(wb[(t) / 20] + ((t) % 20) * 3072 + (n) * 64 + 768)))
#pragma unroll
      for (int t = 0; t < 2; ++t) { RDW(t, 0); RDW(t, 1); RDW(t, 2); }
      const u32x4 xe = {h ? 0u : 0x3F80u, 0u, 0u, 0u};
#pragma unroll
      for (int n = 0; n < 3; ++n) {
        f32x16 z;
#pragma unroll
        for (int i = 0; i < 16; ++i) z[i] = 0.f;
        const u32x4 we = {braw[n], 0u, 0u, 0u};
        acc[n] = MFMA32(asb(we), asb(xe), z);
      }
      SCHED();
#pragma unroll
      for (int t = 0; t < 40; ++t) {
#pragma unroll
        for (int n = 0; n < 3; ++n) {
          acc[n] = MFMA32(wf[t % 3][n], asb(a[t]), acc[n]);
          SCHED();
          if (t == 19 && n == 0) lds_wait(sm, SY_W1, 3);
          if (t + 2 < 40) RDW(t + 2, n);
          if (n == 0 && t >= 1 && t <= 8) LOAD_A(31 + t);
          if (n == 1 && t == 8) lds_post_nw(sm, SY_AISS, lane);
          SCHED();
        }
      }
    }
    lds_post_nw(sm, SY_G1DONE, lane);
    STAMP(4);
    {
      const u32 ao = (l31 >> 4) * 48 * 1024 + (h * 16 + l15) * 16;
      lds_wait(sm, SY_W2RDY, 1);
#pragma unroll
      for (int n = 0; n < 3; ++n)
#pragma unroll
        for (int c2 = 0; c2 < 2; ++c2) {
          float mv[8];
#pragma unroll
          for (int e = 0; e < 8; ++e) {
            const float hv = acc[n][8 * c2 + e];
            const float ex = __builtin_amdgcn_exp2f(hv * 1.44269504089f);
            const float rc = __builtin_amdgcn_rcpf(__builtin_fmaf(ex, ex + 2.f, 2.f));
            mv[e] = hv * __builtin_fmaf(rc, -2.f, 1.f);
            if ((e & 3) == 3) {
              const int q = ((n * 2 + c2) * 2 + (e >> 2)) * BWS;
              SCHED();
#pragma unroll
              for (int u = 0; u < BWS; ++u) if (q + u < 10 * BWS) LOAD_BW1(q + u);
              SCHED();
            }
          }
          u32x4 o = {pk2(mv[0], mv[1]), pk2(mv[2], mv[3]), pk2(mv[4], mv[5]), pk2(mv[6], mv[7])};
          BST(o, ao + n * 1024 + c2 * 512, WS_ACT + ((rp * 8 + gw * 2) * 48 + cs * 3) * 1024);
        }
    }
    lds_post_nw(sm, SY_MISH0 + gw, lane);
    STAMP(5);
    DRAIN();
    SET_FLAG(WS_FC + (((rp * 2 + (gw >> 1)) * 8 + (cs >> 1)) * 4 + (cs & 1) * 2 + (gw & 1)) * 64);
    PRIO(0);
    STAMP(6);
#pragma unroll
    for (int q = 10 * BWS; q < 30; ++q) LOAD_BW1(q);
    poll_flags(p.ws, fcoff, tag);
    STAMP(7);
    LOAD_AF();
    lds_wait(sm, SY_G1DONE, 4);
    gemm2<false>(rs, sm, wave, lane, rt2, nch, bw, af, res);
    STAMP(11);
    const u32 fdoff = WS_FD + ((rt2 >> 4) * 640 + ct2 * 80 + gw * 20 + (lane < 20 ? lane : 0)) * 64;
    const u64 fd = ld_flag(p.ws, fdoff);
    int orow[3], och[3];
    u32x4 xres[3];
#pragma unroll
    for (int i = 0; i < 3; ++i) {
      const int id = (i < 2 || lane < 32) ? lane + 64 * i : lane;
      orow[i] = (id * 205) >> 11; och[i] = id - orow[i] * 10;
      xres[i] = *(const u32x4*)(p.x + (rt2 * 64 + gw * 16 + orow[i]) * 640 + ct2 * 80 + och[i] * 8);
    }
    lds_wait(sm, SY_GPART, 4);
#pragma unroll
    for (int o = 1; o < 4; ++o)
#pragma unroll
      for (int n = 0; n < 5; ++n) res[n] += *(LAS f32x4*)(sm + ((gw - o) & 3) * 15360 + ((o - 1) * 5 + n) * 1024 + l16);
    if (!wall(fd == tag)) poll_flags(p.ws, fdoff, tag);
    CFENCE();
    STAMP(12);
    const u32 yo = (lane >> 3) * 4096 + (lane & 7) * 16;
    u32x4 yv[3];
#pragma unroll
    for (int i = 0; i < 3; ++i) yv[i] = BLD(yo + ((i == 2 && lane >= 32) ? OOB : 0u), WS_YT + (ct2 * 80 + gw * 20 + 8 * i) * 4096 + rt2 * 128);
    lds_wait(sm, SY_CONV, nch + 1);
#pragma unroll
    for (int i = 0; i < 3; ++i) if (i < 2 || lane < 32) *(LAS u32x4*)(sm + LD_X + gw * 2560 + i * 1024 + l16) = yv[i];
    lds_post(sm, SY_YST, lane);
#pragma unroll
    for (int st = 0; st < 3; ++st) {
      const int v = (gw - 1 - st) & 3;
      lds_wait(sm, SY_SP0 + v, st + 1);
#pragma unroll
      for (int n = 0; n < 5; ++n) res[n] += *(LAS f32x4*)(sm + LD_SP + v * 15360 + (st * 5 + n) * 1024 + l16);
    }
    lds_wait(sm, SY_YST, 4);
#pragma unroll
    for (int n = 0; n < 5; ++n) {
      bf16x4 yy = trd(sm + LD_X + (n * 16 + 4 * g + (l15 >> 2)) * 128 + ((lane & 3) >> 1) * 64 + gw * 16 + (lane & 1) * 8);
      u32x2 yu = __builtin_bit_cast(u32x2, yy);
      res[n] += f32x4{bflo(yu.x), bfhi(yu.x), bflo(yu.y), bfhi(yu.y)};
    }
    STAMP(8);
    lds_wait(sm, SY_SP0 + gw, 4);
    STAMP(9);
    LAS char* stg = sm + gw * 15360;
#pragma unroll
    for (int n = 0; n < 5; ++n) *(LAS f32x4*)(stg + l15 * 336 + n * 64 + g * 16) = res[n] + *(LAS f32x4*)(sm + LD_KT + gw * 5120 + n * 1024 + l16);
    CFENCE();
#pragma unroll
    for (int i = 0; i < 3; ++i) {
      f32x4 u = *(LAS f32x4*)(stg + orow[i] * 336 + och[i] * 32), v = *(LAS f32x4*)(stg + orow[i] * 336 + och[i] * 32 + 16);
      u32x4 o = {pk2(u.x + bflo(xres[i].x), u.y + bfhi(xres[i].x)), pk2(u.z + bflo(xres[i].y), u.w + bfhi(xres[i].y)),
                 pk2(v.x + bflo(xres[i].z), v.y + bfhi(xres[i].z)), pk2(v.z + bflo(xres[i].w), v.w + bfhi(xres[i].w))};
      if (i < 2 || lane < 32) *(u32x4*)(p.out + (rt2 * 64 + gw * 16 + orow[i]) * 640 + ct2 * 80 + och[i] * 8) = o;
    }
  } else if (wave == 7) {
    PRIO(PW7);
    u64 ep;
    asm volatile("global_load_dwordx2 %0, %1, %2 sc1" : "=&v"(ep) : "v"(0u), "s"(p.ws + WS_EPOCH) : "memory");
    u32 dsk3 = gld2(p.dsk, (c0 + (lane < nch ? lane : 0)) * 2);
    w1_dma(w1b, smb, 2, 6, lane);
    __builtin_amdgcn_s_barrier();
    asm volatile("s_waitcnt vmcnt(6)" : "+v"(ep), "+v"(dsk3) :: "memory");
    {
      u64 t = ep * 0x9E3779B97F4A7C15ull + 0x632BE59BD9B4E019ull;
      tag = (u64)__builtin_amdgcn_readfirstlane((u32)t) | ((u64)__builtin_amdgcn_readfirstlane((u32)(t >> 32)) << 32);
      if (lane == 0) { lds_st(sm, SY_TAGLO, (u32)tag); lds_st(sm, SY_TAGHI, (u32)(tag >> 32)); LDRAIN(); lds_st(sm, SY_TAGOK, 1); }
    }
    w1_dma(w1b, smb, 8, 40, lane);
    STAMP(1);
    VMW(18);
    lds_post_nw(sm, SY_W1A, lane);
    DRAIN();
    lds_post_nw(sm, SY_W1, lane);
    STAMP(2);
    PRIO(3);
    lds_wait(sm, SY_LN, 2);
    {
      bf16x8 vt[10];
#pragma unroll
      for (int it = 0; it < 10; ++it) {
        LAS char* tp = sm + LD_X + (l15 >> 2) * LNS + (64 * it + 16 * g + 4 * (lane & 3)) * 2;
        vt[it] = cat(trd(tp), trd(tp + 4 * LNS));
      }
      lds_wait(sm, SY_FAOK, GATE);
#pragma unroll
      for (int it = 0; it < 10; ++it) BST(__builtin_bit_cast(u32x4, vt[it]), l16, WS_XNT + (lnb * 640 + 64 * it) * 16);
    }
    DRAIN();
    SET_FLAG(WS_FT + lnb * 64);
    STAMP(12);
    lds_wait(sm, SY_AISS, 4);
    polln<2>(p.ws, WS_FW, lane, 120, tag);
    lds_post_nw(sm, SY_W2RDY, lane);
    STAMP(13);
    polln<4>(p.ws, WS_FT, lane, 256, tag);
    if (b == 0 && lane == 0) __hip_atomic_store((GAS u64*)(p.ws + WS_EPOCH), ep + 1, RLX_AGENT);
    STAMP(3);
    {
      const int chl = lane & 3;
      u32x4 gv[16];
      const u32 go = ((lane >> 2) * 640 + c0 + chl) * 16 + (chl < nch ? 0u : OOB);
#pragma unroll
      for (int it = 0; it < 16; ++it) gv[it] = BLD(go, WS_XNT + it * 16 * 640 * 16);
      if (chl < 3) {
        LAS char* xd = sm + LD_X + chl * X_SZ;
        u32x4 z = {0, 0, 0, 0};
        *(LAS u32x4*)(xd + (lane >> 2) * 16) = z;
        *(LAS u32x4*)(xd + (14 + (lane >> 2)) * 16) = z;
#pragma unroll
        for (int it = 0; it < 16; ++it) *(LAS u32x4*)(xd + (30 + 16 * it + (lane >> 2)) * 16) = gv[it];
      }
    }
    lds_post(sm, SY_XRDY, lane);
    STAMP(4);
    PRIO(0);
    LOAD_BW();
    lds_wait(sm, SY_KRDY, nch);
    const int par = (l15 & 1) ^ 1;
    u32 aB = smb + LD_KT + (par ? KC1 : 0) + 2 * (2031 - l15 + 8 * g - par) - 3584;
    u32 bB = smb + LD_X + 16 * (28 + 2 * l15 + g);
    u32x4 fa[2][4], fb[2][4];
    { const CAddr c = caddr(aB, bB);
#pragma unroll
      for (int q = 0; q < 4; ++q) { fa[0][q] = FA_(c, q); fb[0][q] = FB_(c, q); } }
    lds_wait(sm, SY_MISH3, 1);
    PRIO(1);
    STAMP(5);
    u64 fc = 0;
#pragma unroll 1
    for (int c = 0; c < nch; ++c) {
      f32x4 y[4];
      const u32x4 dg = diag_frag(__builtin_amdgcn_readlane(dsk3, c), l15, g);
#pragma unroll
      for (int i = 0; i < 4; ++i) { f32x4 z = {0, 0, 0, 0}; y[i] = MFMA16(asb(dg), asb(fb[0][i]), z); }
      fc = ld_flag(p.ws, fcoff);
#pragma unroll 1
      for (int rho = 0; rho < 8; rho += 2) {
        if (c && rho == 4) { VMW(0); SET_FLAG(WS_FD + (c0 + c - 1) * 64); }
        conv_lo1(caddr(aB - 64, bB - 64), fa[0], fb[0], fa[1], fb[1], y);
        const bool last = rho == 6, more = c + 1 < nch;
        aB += last ? (more ? KT_SZ + 384 : 384) : -128; bB += last ? (more ? X_SZ + 384 : 384) : -128;
        conv_lo1(caddr(aB, bB), fa[1], fb[1], fa[0], fb[0], y);
      }
      store_y(rs, y, WS_YT + (c0 + c) * 4096, l15, g);
    }
    lds_post(sm, SY_CONV, lane);
    STAMP(6);
    if (!wall(fc == tag)) poll_flags(p.ws, fcoff, tag);
    CFENCE();
    LOAD_AF();
    VMW(18);
    SET_FLAG(WS_FD + (c0 + nch - 1) * 64);
    PRIO(0);
    STAMP(7);
    lds_wait(sm, SY_G1DONE, 4);
    gemm2<true>(rs, sm, wave, lane, rt2, nch, bw, af, res);
  } else {
    const int s = wave - 4;
    u32 tch[3];
    const bool hasch = s < nch;
    const int ch = hasch ? c0 + s : c0;
    LAS char* KT = sm + LD_KT + s * KT_SZ;
    { u32 t0 = gld2(p.b1, 0), t1 = gld2(p.out, 0), t2_ = gld2(p.w2, 0);
      asm volatile("" :: "v"(t0), "v"(t1), "v"(t2_)); tch[0] = t0; tch[1] = t1; tch[2] = t2_; }
    u32 prm[6][3];
    const u32 po = ch * 384 + lane * 2;
    u32 ldt_raw = gld2(p.ldt, ch * 2), dsk_raw = gld2(p.dsk, ch * 2);
#pragma unroll
    for (int it = 0; it < 3; ++it) {
      prm[0][it] = gld2(p.Are, po + it * 128); prm[1][it] = gld2(p.Aim, po + it * 128); prm[2][it] = gld2(p.Bre, po + it * 128);
      prm[3][it] = gld2(p.Bim, po + it * 128); prm[4][it] = gld2(p.Cre, po + it * 128); prm[5][it] = gld2(p.Cim, po + it * 128);
    }
    __builtin_amdgcn_s_barrier();
    if (!hasch) {
      const int t2 = xcd * 16 + (j >> 1);
      PRIO(3);
      u32x4 wr[2][8];
      const u32 wo = g * 1280 + l15 * 16 + 3072;
#pragma unroll
      for (int pc = 0; pc < 2; ++pc) {
        const int pid = (t2 < 120 ? 2 * t2 : 0) + pc, rks = pid / 5, cb = pid - rks * 5;
        const char* src = (const char*)p.w2 + (32 * rks * 640 + 128 * cb) * 2 - 3072;
#pragma unroll
        for (int i = 0; i < 2; ++i) {
          setm0((u32)(size_t)KT + i * 4096);
          glds<0>(src + i * 20480, wo); glds<1024>(src + i * 20480 + 4096, wo); glds<2048>(src + i * 20480 + 8192, wo); glds<3072>(src + i * 20480 + 12288, wo);
        }
        DRAIN();
#pragma unroll
        for (int n = 0; n < 8; ++n) {
          LAS char* tp = KT + (16 * (g >> 1) + 4 * (g & 1) + (l15 >> 2)) * 256 + 32 * n + 8 * (lane & 3);
          wr[pc][n] = __builtin_bit_cast(u32x4, cat(trd(tp), trd(tp + 2048)));
        }
        LDRAIN();
      }
      LDRAIN(); STAMP(8);
      tag = get_tag(sm);
      lds_wait(sm, SY_FAOK, GATEW);
#pragma unroll
      for (int pc = 0; pc < 2; ++pc) {
        const int pid = 2 * t2 + pc, rks = pid / 5, cb = pid - rks * 5;
#pragma unroll
        for (int n = 0; n < 8; ++n) BST(wr[pc][n], l16 + n * 1024 + (t2 < 120 ? 0u : OOB), WS_W2F + (rks * 40 + 8 * cb) * 1024);
      }
      STAMP(9);
      DRAIN();
      STAMP(11);
      SET_FLAG(WS_FW + t2 * 64);
      PRIO(0);
    }
    asm volatile("s_waitcnt vmcnt(0)" : "+v"(ldt_raw), "+v"(dsk_raw), "+v"(tch[0]), "+v"(tch[1]), "+v"(tch[2]) :: "memory");
#pragma unroll
    for (int k = 0; k < 6; ++k) asm volatile("" : "+v"(prm[k][0]), "+v"(prm[k][1]), "+v"(prm[k][2]));
    SCHED();
    STAMP(1);
    if (hasch) {
      const int m = l31;
      const float dt = rbf(__builtin_amdgcn_exp2f(bflo(ldt_raw) * 1.44269504089f));
      cf cc[3]; float mx = 0.f;
#pragma unroll
      for (int it = 0; it < 3; ++it) {
        const cf Bc = {bflo(prm[2][it]), bflo(prm[3][it])}, Cc = {bflo(prm[4][it]), bflo(prm[5][it])};
        cc[it] = cmul(Cc, Bc);
        mx = __builtin_fmaxf(mx, __builtin_fmaxf(__builtin_fabsf(cc[it].x), __builtin_fabsf(cc[it].y)));
      }
      const int ex = __builtin_amdgcn_frexp_expf(max64(mx));
      const float ksc = __builtin_amdgcn_ldexpf(dt, ex - 10);
      f32x16 kacc;
#pragma unroll
      for (int i = 0; i < 16; ++i) kacc[i] = 0.f;
      const int baseA = 512 * h + 16 * ((m >> 2) ^ (h << 1)) + 4 * (m & 3);
      const int baseB = 4096 + 2048 * h + 16 * (m >> 2) + 4 * (m & 3);
      const int rdA = m * 128, rsw = (m >> 1) & 7;
      u32x4 ta[4], tb[4];
#define KMFMA(ks) kacc = MFMA32H(__builtin_bit_cast(f16x8, ta[ks]), __builtin_bit_cast(f16x8, tb[ks]), kacc)
#define WRA(rr, v) (*(LAS u32*)(KT + ((baseA ^ (16 * ((((rr) & 3) >> 1) | ((((rr) >> 2) & 1) << 2)))) + 128 * (((rr) & 3) + 8 * ((rr) >> 2)))) = (v))
#define WRB(be, v) (*(LAS u32*)(KT + ((baseB ^ (16 * ((be) >> 1))) + 128 * (be))) = (v))
#define SWAP(dst, a0, a1) do { auto q_ = __builtin_amdgcn_permlane32_swap((a0), (a1), false, false); dst[0] = q_[0]; dst[1] = q_[1]; } while (0)
#pragma unroll
      for (int it = 0; it < 3; ++it) {
        const float lr = dt * bflo(prm[0][it]) * 1.44269504089f, ph = dt * bflo(prm[1][it]) * 0.15915494309f;
        const cf c = {__builtin_amdgcn_ldexpf(cc[it].x, 10 - ex), __builtin_amdgcn_ldexpf(cc[it].y, 10 - ex)};
        const cf e1 = cexp2r(lr, ph), e128 = cexp2r(lr * 128.f, ph * 128.f);
        const cf e2 = csq(e1), e256 = csq(e128), e4 = csq(e2), e512 = csq(e256);
        SCHED(); if (it) KMFMA(0); SCHED();
        const cf e8 = csq(e4), e1024 = csq(e512), e16 = csq(e8), e32 = csq(e16);
        SCHED(); if (it) KMFMA(1); SCHED();
        const cf e64 = csq(e32), c512 = cmul(c, e512), c1024 = cmul(c, e1024);
        const u32 g1 = toh(e1), g2 = toh(e2), g4 = toh(e4), g8 = toh(e8), g16 = toh(e16);
        SCHED(); if (it) KMFMA(2); SCHED();
        const u32 g256 = toh(e256), g512 = toh(e512), k32 = tohc(e32), k64 = tohc(e64), k128 = tohc(e128);
        const u32 g3 = hmul(g2, g1), g24 = hmul(g16, g8), k96 = hmul(k64, k32);
        const u32 b0 = toh(c), b7 = toh(c512), b10 = toh(c1024);
        SCHED(); if (it) KMFMA(3); SCHED();
        u32 R0[2], R7[2], R10[2], P[4][2], H1[2], H2[2], H3[2], H4[2], H8[2], H16[2], H24[2], H256[2];
        SWAP(R0, b0, hmul(b0, g4)); SWAP(R7, b7, hmul(b7, g256)); SWAP(R10, b10, hmul(b10, g512));
        SWAP(P[0], 0x00003C00u, k128); SWAP(P[1], k32, hmul(k128, k32)); SWAP(P[2], k64, hmul(k128, k64)); SWAP(P[3], k96, hmul(k128, k96));
        SWAP(H1, g1, g1); SWAP(H2, g2, g2); SWAP(H3, g3, g3); SWAP(H4, g4, g4); SWAP(H8, g8, g8); SWAP(H16, g16, g16); SWAP(H24, g24, g24); SWAP(H256, g256, g256);
        SCHED();
#pragma unroll
        for (int sp = 0; sp < 2; ++sp) {
#pragma unroll
          for (int a = 0; a < 4; ++a) {
            WRB(4 * a, P[a][sp]); WRB(4 * a + 1, hmulc(P[a][sp], H8[sp])); SCHED();
            WRB(4 * a + 2, hmulc(P[a][sp], H16[sp])); WRB(4 * a + 3, hmulc(P[a][sp], H24[sp])); SCHED();
            if (sp) { KMFMA(a); SCHED(); }
          }
          const u32 r4 = hmul(R0[sp], H256[sp]), r12 = hmul(R10[sp], H256[sp]);
          WRA(0, R0[sp]); WRA(1, hmul(R0[sp], H1[sp])); SCHED();
          WRA(2, hmul(R0[sp], H2[sp])); WRA(3, hmul(R0[sp], H3[sp])); SCHED();
          WRA(4, r4); WRA(5, hmul(r4, H2[sp])); SCHED();
          WRA(6, hmul(r4, H4[sp])); WRA(7, R7[sp]); SCHED();
          WRA(8, hmul(R7[sp], H4[sp])); WRA(9, hmul(R7[sp], H8[sp])); SCHED();
          WRA(10, R10[sp]); WRA(11, hmul(R10[sp], H8[sp])); SCHED();
          WRA(12, r12); WRA(13, hmul(r12, H8[sp]));
          CFENCE(); SCHED();
#pragma unroll
          for (int ks = 0; ks < 4; ++ks) {
            const int so = rdA + 16 * ((2 * ks + h) ^ rsw);
            ta[ks] = *(LAS u32x4*)(KT + so); tb[ks] = *(LAS u32x4*)(KT + 4096 + so);
          }
          CFENCE(); SCHED();
        }
      }
      KMFMA(0); KMFMA(1); KMFMA(2); KMFMA(3);
      CFENCE();
      STAMP(2);
      {
        const int bb = m;
        float kv[14];
#pragma unroll
        for (int i = 0; i < 14; ++i) kv[i] = kacc[i] * ksc;
        u32x2 s0 = {pk2(kv[3], kv[2]), pk2(kv[1], kv[0])};
        *(LAS u32x2*)(KT + 2 * (2044 - 8 * bb - 4 * h)) = s0;
        u32x2 s1 = {pk2(kv[6], kv[5]), pk2(kv[4], kv[4])};
        *(LAS u32x2*)(KT + 2 * (2044 - 256 - 8 * bb - 4 * h)) = s1;
        u32x4 s2 = {pk2(kv[9], kv[9]), pk2(kv[8], kv[8]), pk2(kv[8], kv[7]), pk2(kv[7], kv[7])};
        *(LAS u32x4*)(KT + 2 * (2040 - 512 - 256 * h - 8 * bb)) = s2;
#pragma unroll
        for (int a2 = 0; a2 < 2; ++a2) {
          const float v0 = kv[10 + 2 * a2], v8 = kv[11 + 2 * a2], tie = (bb & 1) ? v8 : v0;
          u32x4 s3 = {pk2(v8, v8), pk2(v8, tie), pk2(v0, v0), pk2(v0, v0)};
          *(LAS u32x4*)(KT + 2 * (2040 - 1024 - 512 * h - 256 * a2 - 8 * bb)) = s3;
        }
        u32x4 z = {0, 0, 0, 0};
        if (lane < 4) *(LAS u32x4*)(KT + 4096 + (lane & 1) * 16 + (lane >> 1) * KC1) = z;
        CFENCE();
        u32 dw[17];
#pragma unroll
        for (int k = 0; k < 4; ++k) { u32x4 v = *(LAS u32x4*)(KT + lane * 64 + k * 16); dw[4 * k] = v.x; dw[4 * k + 1] = v.y; dw[4 * k + 2] = v.z; dw[4 * k + 3] = v.w; }
        dw[16] = *(LAS u32*)(KT + lane * 64 + 64);
#pragma unroll
        for (int k = 0; k < 4; ++k) {
          u32x4 v;
#pragma unroll
          for (int e = 0; e < 4; ++e) v[e] = __builtin_amdgcn_alignbit(dw[4 * k + e + 1], dw[4 * k + e], 16);
          *(LAS u32x4*)(KT + KC1 + lane * 64 + k * 16) = v;
        }
      }
      lds_post(sm, SY_KRDY, lane);
    }
    STAMP(3);
    tag = get_tag(sm);
    lds_wait(sm, SY_W2RDY, 1);
    LOAD_BW();
    if (hasch) {
      f32x4 y[4];
      const int par = (l15 & 1) ^ 1;
      u32 aB = (u32)(size_t)KT + (par ? KC1 : 0) + 2 * (2031 - l15 + 8 * g - par) - 3584;
      u32 bB = smb + LD_X + s * X_SZ + 16 * (28 + 2 * l15 + g);
      u32x4 fa[8], fb[8];
      lds_wait(sm, SY_XRDY, 1);
      { const CAddr c = caddr(aB, bB);
#pragma unroll
        for (int q = 0; q < 8; ++q) { fb[7 - q] = FB_(c, 7 - q); fa[q] = FA_(c, q); } }
      const u32x4 dg = diag_frag(dsk_raw, l15, g);
      SCHED();
      lds_wait(sm, SY_MISH0 + s, 1);
      PRIO(1);
      STAMP(4);
#pragma unroll
      for (int i = 0; i < 4; ++i) { f32x4 z = {0, 0, 0, 0}; y[i] = MFMA16(asb(dg), asb(fb[4 + i]), z); }
      u64 fc = 0;
#pragma unroll 1
      for (int rho = 0; rho < 8; rho += 2) {
        fc = ld_flag(p.ws, fcoff);
        const u32 nx = rho < 6 ? 128 : 0;
        conv_hi2(caddr(aB - 64, bB - 64), caddr(aB - nx, bB - nx), fa, fb, y); aB -= 128; bB -= 128;
      }
      STAMP(5);
      store_y(rs, y, WS_YT + ch * 4096 + 2048, l15, g);
      lds_post(sm, SY_CONV, lane);
      STAMP(6);
      if (!wall(fc == tag)) poll_flags(p.ws, fcoff, tag);
      CFENCE();
      LOAD_AF();
      VMW(18);
      SET_FLAG(WS_FD + (640 + ch) * 64);
      PRIO(0);
      STAMP(7);
    } else {
      lds_wait(sm, SY_MISH0 + s, 1);
      poll_flags(p.ws, fcoff, tag);
      LOAD_AF();
    }
    lds_wait(sm, SY_G1DONE, 4);
    gemm2<true>(rs, sm, wave, lane, rt2, nch, bw, af, res);
  }
  STAMP(10);
}

extern "C" void kernel_launch(void* const* d_in, const int* in_sizes, int n_in, void* d_out, int out_size,
                              void* d_ws, size_t ws_size, hipStream_t stream) {
  Params p;
  p.x = (const u16*)d_in[0]; p.Are = (const u16*)d_in[1]; p.Aim = (const u16*)d_in[2]; p.Bre = (const u16*)d_in[3];
  p.Bim = (const u16*)d_in[4]; p.Cre = (const u16*)d_in[5]; p.Cim = (const u16*)d_in[6]; p.ldt = (const u16*)d_in[7];
  p.dsk = (const u16*)d_in[8]; p.w1 = (const u16*)d_in[10]; p.b1 = (const u16*)d_in[11]; p.w2 = (const u16*)d_in[12];
  p.out = (u16*)d_out; p.ws = (char*)d_ws;
  (void)hipFuncSetAttribute((const void*)fused, hipFuncAttributeMaxDynamicSharedMemorySize, LDS_BYTES);
  fused<<<256, 512, LDS_BYTES, stream>>>(p);
}
```

```cpp
#include <hip/hip_runtime.h>
#include <stdint.h>
typedef unsigned short u16; typedef unsigned u32; typedef unsigned long long u64;
typedef __attribute__((ext_vector_type(2))) float f32x2;
typedef __attribute__((ext_vector_type(4))) float f32x4;
typedef __attribute__((ext_vector_type(16))) float f32x16;
typedef __attribute__((ext_vector_type(2))) __bf16 bf16x2;
typedef __attribute__((ext_vector_type(4))) __bf16 bf16x4;
typedef __attribute__((ext_vector_type(8))) __bf16 bf16x8;
typedef __attribute__((ext_vector_type(2))) _Float16 f16x2;
typedef __attribute__((ext_vector_type(8))) _Float16 f16x8;
typedef __attribute__((ext_vector_type(2))) u32 u32x2;
typedef __attribute__((ext_vector_type(3))) u32 u32x3;
typedef __attribute__((ext_vector_type(4))) u32 u32x4;
#define LAS __attribute__((address_space(3)))
#define GAS __attribute__((address_space(1)))
#define RLX_AGENT __ATOMIC_RELAXED, __HIP_MEMORY_SCOPE_AGENT
#define RLX_WG __ATOMIC_RELAXED, __HIP_MEMORY_SCOPE_WORKGROUP
#if defined(__HIP_DEVICE_COMPILE__)
#define NOPK __attribute__((target("no-packed-fp32-ops")))
#else
#define NOPK
#endif
#define DEV __device__ __forceinline__ NOPK
#define CFENCE() asm volatile("" ::: "memory")
#define DRAIN() asm volatile("s_waitcnt vmcnt(0)" ::: "memory")
#define LDRAIN() asm volatile("s_waitcnt lgkmcnt(0)" ::: "memory")
#define VMW(n) asm volatile("s_waitcnt vmcnt(%0)" :: "i"(n) : "memory")
#define SCHED() __builtin_amdgcn_sched_barrier(0)
#define PRIO(n) __builtin_amdgcn_s_setprio(n)
#define DMA_G 14
#define BWS 1
#define G2MAP 0
#define GATE 0
#define GATEW 0
#define PW7 1
#define PSLEEP 4

constexpr int L_ = 2048, D_ = 640, N_ = 192, F_ = 1536;
constexpr u32 WS_EPOCH = 0;
constexpr u32 WS_FT = 4096;
constexpr u32 WS_FW = WS_FT + 256 * 64;
constexpr u32 WS_FD = WS_FW + 128 * 64;
constexpr u32 WS_FC = WS_FD + 1280 * 64;
constexpr u32 WS_FA = WS_FC + 1024 * 64;
constexpr u32 WS_XNA = WS_FA + 512 * 64;
constexpr u32 WS_XNT = WS_XNA + L_ * D_ * 2;
constexpr u32 WS_W2F = WS_XNT + L_ * D_ * 2;
constexpr u32 WS_ACT = WS_W2F + F_ * D_ * 2;
constexpr u32 WS_YT = WS_ACT + L_ * F_ * 2;
constexpr u32 WS_END = WS_YT + L_ * D_ * 2;
constexpr u32 WS_STAMP = WS_END;
constexpr u32 OOB = 0x40000000u;
constexpr int KT_SZ = 8320, KC1 = 4160, X_SZ = 4608;
constexpr int LD_KT = 122880, LD_X = LD_KT + 3 * KT_SZ;
constexpr int LD_SYNC = LD_X + 3 * X_SZ;
constexpr int LDS_BYTES = 163840;
constexpr int LD_SP = 61440;
constexpr int LNS = 1296;
enum { SY_W1 = 0, SY_G1DONE, SY_W2RDY, SY_TAGLO, SY_TAGHI, SY_TAGOK, SY_KRDY, SY_CONV, SY_MISH0, SY_MISH1, SY_MISH2, SY_MISH3,
       SY_XRDY, SY_LN, SY_FAOK, SY_AISS, SY_GPART, SY_YST, SY_SP0, SY_SP1, SY_SP2, SY_SP3, SY_W1A };
constexpr u32 SPIN_CAP = 1u << 18;

struct Params {
  const u16 *x, *Are, *Aim, *Bre, *Bim, *Cre, *Cim, *ldt, *dsk, *w1, *b1, *w2;
  u16* out; char* ws;
};

DEV float asf(u32 v) { return __builtin_bit_cast(float, v); }
DEV u32 asu(float v) { return __builtin_bit_cast(u32, v); }
DEV float bflo(u32 v) { return asf(v << 16); }
DEV float bfhi(u32 v) { return asf(v & 0xffff0000u); }
DEV u32 pk2(float a, float b) { f32x2 v = {a, b}; return __builtin_bit_cast(u32, __builtin_convertvector(v, bf16x2)); }
DEV float rbf(float a) { return bflo(pk2(a, a)); }
struct cf { float x, y; };
DEV cf cmul(cf a, cf b) { cf r = {__builtin_fmaf(-a.y, b.y, a.x * b.x), __builtin_fmaf(a.y, b.x, a.x * b.y)}; return r; }
DEV cf cmulc(cf a, cf b) { cf r = {__builtin_fmaf(a.y, b.y, a.x * b.x), __builtin_fmaf(a.y, b.x, -(a.x * b.y))}; return r; }
DEV cf csq(cf a) { cf r = {__builtin_fmaf(-a.y, a.y, a.x * a.x), (a.x + a.x) * a.y}; return r; }
DEV cf cexp2r(float lr, float ph) {
  float e = __builtin_amdgcn_exp2f(lr); float f = __builtin_amdgcn_fractf(ph);
  cf r = {e * __builtin_amdgcn_cosf(f), e * __builtin_amdgcn_sinf(f)}; return r; }
DEV cf csel(bool h, cf a) { cf r = {h ? a.x : 1.f, h ? a.y : 0.f}; return r; }
DEV u32 toh(cf a) { f32x2 v = {a.x, a.y}; return __builtin_bit_cast(u32, __builtin_convertvector(v, f16x2)); }
DEV u32 tohc(cf a) { f32x2 v = {a.x, -a.y}; return __builtin_bit_cast(u32, __builtin_convertvector(v, f16x2)); }
DEV u32 hmul(u32 a_, u32 b_) {
  const f16x2 a = __builtin_bit_cast(f16x2, a_), b = __builtin_bit_cast(f16x2, b_), nb = -b;
  const f16x2 s = __builtin_shufflevector(nb, b, 1, 2);
  return __builtin_bit_cast(u32, __builtin_elementwise_fma(a.yy, s, a.xx * b)); }
DEV u32 hmulc(u32 a_, u32 b_) {
  const f16x2 a = __builtin_bit_cast(f16x2, a_), b = __builtin_bit_cast(f16x2, b_), nb = -b;
  const f16x2 s = __builtin_shufflevector(b, nb, 0, 3);
  return __builtin_bit_cast(u32, __builtin_elementwise_fma(a.yy, b.yx, a.xx * s)); }
template <int CTRL> DEV float dppf(float v) { return asf(__builtin_amdgcn_update_dpp(0, asu(v), CTRL, 0xf, 0xf, true)); }
DEV float red8(float v) { v += dppf<0xB1>(v); v += dppf<0x4E>(v); v += dppf<0x141>(v); return v; }
DEV float red_mod4(float v) {
  v += dppf<0x124>(v); v += dppf<0x128>(v);
  auto r = __builtin_amdgcn_permlane16_swap(asu(v), asu(v), false, false);
  v = asf(r[0]) + asf(r[1]);
  auto q = __builtin_amdgcn_permlane32_swap(asu(v), asu(v), false, false);
  return asf(q[0]) + asf(q[1]); }
DEV float max64(float v) {
  v = __builtin_fmaxf(v, dppf<0xB1>(v)); v = __builtin_fmaxf(v, dppf<0x4E>(v)); v = __builtin_fmaxf(v, dppf<0x141>(v)); v = __builtin_fmaxf(v, dppf<0x140>(v));
  auto r = __builtin_amdgcn_permlane16_swap(asu(v), asu(v), false, false);
  v = __builtin_fmaxf(asf(r[0]), asf(r[1]));
  auto q = __builtin_amdgcn_permlane32_swap(asu(v), asu(v), false, false);
  return __builtin_fmaxf(asf(q[0]), asf(q[1])); }
DEV bool wall(bool c) { return __builtin_amdgcn_ballot_w64(c) == ~0ull; }

DEV u32 lds_ld(LAS char* s, int w) { return __hip_atomic_load((LAS u32*)(s + LD_SYNC) + w, RLX_WG); }
DEV void lds_st(LAS char* s, int w, u32 v) { __hip_atomic_store((LAS u32*)(s + LD_SYNC) + w, v, RLX_WG); }
DEV void lds_wait(LAS char* s, int w, u32 want) {
#pragma unroll 1
  for (u32 n = 0; n < (1u << 22); ++n) { if (__builtin_amdgcn_readfirstlane(lds_ld(s, w)) >= want) break; __builtin_amdgcn_s_sleep(1); }
  CFENCE();
}
DEV void lds_post(LAS char* s, int w, int lane) {
  LDRAIN();
  if (lane == 0) (void)__hip_atomic_fetch_add((LAS u32*)(s + LD_SYNC) + w, 1u, RLX_WG);
  CFENCE();
}
DEV void lds_post_nw(LAS char* s, int w, int lane) {
  CFENCE();
  if (lane == 0) (void)__hip_atomic_fetch_add((LAS u32*)(s + LD_SYNC) + w, 1u, RLX_WG);
  CFENCE();
}
DEV u64 get_tag(LAS char* s) {
  lds_wait(s, SY_TAGOK, 1);
  return (u64)__builtin_amdgcn_readfirstlane(lds_ld(s, SY_TAGLO)) | ((u64)__builtin_amdgcn_readfirstlane(lds_ld(s, SY_TAGHI)) << 32);
}
DEV u64 ld_flag(char* ws, u32 off) { return __hip_atomic_load((GAS u64*)(ws + off), RLX_AGENT); }
DEV void poll_flags(char* ws, u32 off, u64 tag) {
#pragma unroll 1
  for (u32 n = 0; n < SPIN_CAP; ++n) {
    if (wall(ld_flag(ws, off) == tag)) break;
    __builtin_amdgcn_s_sleep(PSLEEP);
  }
  CFENCE();
}
template <int NL> DEV void polln(char* ws, u32 base, int lane, int nvalid, u64 tag) {
#pragma unroll 1
  for (u32 n = 0; n < SPIN_CAP; ++n) {
    bool ok = true;
#pragma unroll
    for (int k = 0; k < NL; ++k) { int i = lane + 64 * k; u64 v = ld_flag(ws, base + i * 64); ok &= (v == tag) | (i >= nvalid); }
    if (wall(ok)) break;
    __builtin_amdgcn_s_sleep(16);
  }
  CFENCE();
}
DEV bf16x4 trd(LAS char* p) { return __builtin_amdgcn_ds_read_tr16_b64_v4bf16((LAS bf16x4*)p); }
DEV bf16x8 cat(bf16x4 a, bf16x4 b) { return __builtin_shufflevector(a, b, 0, 1, 2, 3, 4, 5, 6, 7); }
DEV bf16x8 asb(u32x4 v) { return __builtin_bit_cast(bf16x8, v); }
DEV LAS char* lp(u32 a) { return (LAS char*)(size_t)a; }
#define MFMA16(a, b, c) __builtin_amdgcn_mfma_f32_16x16x32_bf16(a, b, c, 0, 0, 0)
#define MFMA32(a, b, c) __builtin_amdgcn_mfma_f32_32x32x16_bf16(a, b, c, 0, 0, 0)
#define MFMA32H(a, b, c) __builtin_amdgcn_mfma_f32_32x32x16_f16(a, b, c, 0, 0, 0)
#define BLD(voff, soff) __builtin_amdgcn_raw_buffer_load_b128(rs, (voff), (soff), 16)
#define BST(v, voff, soff) __builtin_amdgcn_raw_buffer_store_b128((v), rs, (voff) + (soff), 0, 16)

#define SET_FLAG(off) do { const u32x4 f_ = {(u32)tag, (u32)(tag >> 32), (u32)tag, (u32)(tag >> 32)}; BST(f_, l16 + (lane < 4 ? 0u : OOB), (off)); } while (0)

#define STAMP(i)

DEV void setm0(u32 v) { asm volatile("s_mov_b32 m0, %0\n\ts_nop 0" :: "s"(v) : "memory"); }
template <int OFF> DEV void glds(const void* sbase, u32 voff) {
  asm volatile("global_load_lds_dwordx4 %0, %1 offset:%2" :: "v"(voff), "s"(sbase), "i"(OFF) : "memory");
}
DEV void w1_dma(const char* wb, u32 smb, int first, int last, int lane) {
  u32 so[3];
#pragma unroll
  for (int r = 0; r < 3; ++r) { const int c = r * 64 + lane, row = c / 12, cc = c - row * 12; so[r] = row * 3072 + cc * 16 + 2048 - 1024 * r; }
#pragma unroll 1
  for (int i = first; i < last; i += 3) {
    const char* src = wb + i * 16 * 3072 - 2048;
    setm0(smb + i * 3072);
    glds<0>(src, so[0]); glds<1024>(src, so[1]); glds<2048>(src, so[2]);
  }
}
DEV u32x4 ldA(u32 a) { LAS u32* ap = (LAS u32*)lp(a); u32x4 v = {ap[0], ap[1], ap[2], ap[3]}; return v; }
DEV u32x4 ldB(u32 a) { return *(LAS u32x4*)lp(a); }
struct CAddr { u32 a[4]; u32 b; };
DEV CAddr caddr(u32 aB, u32 bB) {
  CAddr c;
#pragma unroll
  for (int i = 0; i < 4; ++i) { c.a[i] = aB + 1024 * (3 - i); asm("" : "+v"(c.a[i])); }
  c.b = bB; asm("" : "+v"(c.b));
  return c;
}
#define FA_(c, q) ldA((c).a[(q) >> 1] + (((q) & 1) ? 0 : 512))
#define FB_(c, q) ldB((c).b + 512 * (q))
DEV void conv_hi2(const CAddr& n1, const CAddr& n2, u32x4 (&fa)[8], u32x4 (&fb)[8], f32x4 (&y)[4]) {
#pragma unroll
  for (int q = 0; q < 8; ++q) {
#pragma unroll
    for (int pp = 7 - q; pp >= 0 && pp + q >= 4; --pp) {
      y[q + pp - 4] = MFMA16(asb(fa[q]), asb(fb[pp]), y[q + pp - 4]);
      if (pp == 7 - q) { SCHED(); fb[pp] = FB_(n1, pp); SCHED(); }
    }
    SCHED(); fa[q] = FA_(n1, q); SCHED();
  }
#pragma unroll
  for (int pp = 7; pp >= 0; --pp) {
#pragma unroll
    for (int q = (pp < 4 ? 4 - pp : 0); q <= 7 - pp; ++q) {
      y[q + pp - 4] = MFMA16(asb(fa[q]), asb(fb[pp]), y[q + pp - 4]);
      if (pp == (q < 4 ? 4 - q : 0)) { SCHED(); fa[q] = FA_(n2, q); SCHED(); }
    }
    SCHED(); fb[pp] = FB_(n2, pp); SCHED();
  }
}
DEV void conv_lo1(const CAddr& n, const u32x4 (&ca)[4], const u32x4 (&cb)[4], u32x4 (&na)[4], u32x4 (&nb)[4], f32x4 (&y)[4]) {
  int k = 0;
#pragma unroll
  for (int q = 0; q < 4; ++q)
#pragma unroll
    for (int pp = 3 - q; pp >= 0; --pp) {
      y[q + pp] = MFMA16(asb(ca[q]), asb(cb[pp]), y[q + pp]);
      SCHED();
      if (k < 8) { if (k & 1) nb[3 - (k >> 1)] = FB_(n, 3 - (k >> 1)); else na[k >> 1] = FA_(n, k >> 1); }
      ++k;
      SCHED();
    }
}
DEV u32x4 diag_frag(u32 dsk, int l15, int g) {
  const u32 v = (g == 2 + (l15 >> 3)) ? ((l15 & 1) ? dsk << 16 : dsk) : 0u;
  const int w = (l15 & 7) >> 1;
  u32x4 r = {w == 0 ? v : 0u, w == 1 ? v : 0u, w == 2 ? v : 0u, w == 3 ? v : 0u};
  return r;
}
DEV void store_y(const __amdgpu_buffer_rsrc_t rs, const f32x4 (&y)[4], u32 base, int l15, int g) {
#pragma unroll
  for (int t = 0; t < 4; t += 2) {
    const u32 a0 = pk2(y[t].x, y[t].y), a1 = pk2(y[t].z, y[t].w), b0 = pk2(y[t + 1].x, y[t + 1].y), b1 = pk2(y[t + 1].z, y[t + 1].w);
    auto s0 = __builtin_amdgcn_permlane16_swap(a0, b0, false, false);
    auto s1 = __builtin_amdgcn_permlane16_swap(a1, b1, false, false);
    const u32x4 o = {s0[0], s1[0], s0[1], s1[1]};
    BST(o, (t + (g & 1)) * 512 + (l15 >> 2) * 128 + (g >> 1) * 64 + (l15 & 3) * 16, base);
  }
}
DEV u32 gld2(const void* sbase, u32 voff) { u32 r; asm volatile("global_load_ushort %0, %1, %2" : "=&v"(r) : "v"(voff), "s"(sbase) : "memory"); return r; }

template <bool SW>
DEV void gemm2(const __amdgpu_buffer_rsrc_t rs, LAS char* sm, int wave, int lane, int rt2, int nch,
               u32x4 (&bw)[6][5], u32x4 (&af)[4][6], f32x4 (&res)[5]) {
  const int own = wave & 3;
  const u32 l16 = lane * 16;
  LAS char* dst = sm + wave * 15360 + l16;
  f32x4 acc[2][5];
#pragma unroll
  for (int st = 0; st < 4; ++st) {
#pragma unroll
    for (int k = 0; k < 6; ++k) {
#pragma unroll
      for (int n = 0; n < 5; ++n) {
        f32x4 z = {0, 0, 0, 0};
        acc[st & 1][n] = MFMA16(asb(bw[k][n]), asb(af[st][k]), k ? acc[st & 1][n] : z);
      }
      SCHED();
      if (st == 0) af[3][k] = BLD(l16 + (k & 3) * 1024, WS_ACT + ((rt2 * 4 + own) * 48 + wave * 6 + (k & 4)) * 1024);
      if (st > 0 && k < 5) *(LAS f32x4*)(dst + ((st - 1) * 5 + k) * 1024) = acc[(st - 1) & 1][k];
      if (st > 0 && k == 5) { if (SW) lds_post_nw(sm, SY_SP0 + own, lane); else if (st == 3) lds_post_nw(sm, SY_GPART, lane); }
      SCHED();
    }
  }
  if (SW) {
    lds_wait(sm, SY_CONV, nch + 1);
#pragma unroll
    for (int n = 0; n < 5; ++n) *(LAS f32x4*)(sm + LD_KT + own * 5120 + n * 1024 + l16) = acc[1][n];
    lds_post_nw(sm, SY_SP0 + own, lane);
  } else {
#pragma unroll
    for (int n = 0; n < 5; ++n) res[n] = acc[1][n];
  }
}

__global__ __launch_bounds__(512, 2) NOPK void fused(Params p) {
  extern __shared__ __attribute__((aligned(16))) char smem_[];
  LAS char* sm = (LAS char*)smem_;
  const u32 smb = (u32)(size_t)sm;
  const int b = __builtin_amdgcn_workgroup_id_x(), xcd = b & 7, j = b >> 3;
  const int tid = __builtin_amdgcn_workitem_id_x(), wave = __builtin_amdgcn_readfirstlane(tid >> 6), lane = tid & 63;
  const int g = lane >> 4, l15 = lane & 15, l31 = lane & 31, h = lane >> 5;
  const u32 l16 = lane * 16;
  const __amdgpu_buffer_rsrc_t rs = __builtin_amdgcn_make_buffer_rsrc(p.ws, 0, WS_END, 0x00020000);
  const u64 t_begin = __builtin_amdgcn_s_memrealtime();
  const int rt2 = xcd * 4 + (j & 3), ct2 = j >> 2;
  const int rp = (xcd & 3) * 4 + (j & 3), cs = (xcd >> 2) * 8 + (j >> 2);
  const int lnb = xcd * 32 + j;
  const int c0 = xcd * 80 + (j >> 1) * 5 + ((j & 1) ? 3 : 0), nch = (j & 1) ? 2 : 3;
  const int own = wave & 3;
  const u32 fcoff = WS_FC + ((rt2 * 8 + wave) * 4 + (lane & 3)) * 64;
  const char* w1b = (const char*)p.w1 + cs * 192;
  u64 tag;
  f32x4 res[5];
  u32x4 bw[6][5], af[4][6];
#define LOAD_BW1(q) bw[(q) / 5][(q) % 5] = BLD(l16 + (((q) % 5) & 3) * 1024, WS_W2F + ((wave * 6 + (q) / 5) * 40 + ct2 * 5 + (((q) % 5) & 4)) * 1024)
#define LOAD_BW() do { _Pragma("unroll") for (int q = 0; q < 30; ++q) LOAD_BW1(q); SCHED(); } while (0)
#define LOAD_AF() do { _Pragma("unroll") for (int st = 0; st < 3; ++st) _Pragma("unroll") for (int k = 0; k < 6; ++k) \
    af[st][k] = BLD(l16 + (k & 3) * 1024, WS_ACT + ((rt2 * 4 + ((own + 1 + st) & 3)) * 48 + wave * 6 + (k & 4)) * 1024); SCHED(); } while (0)

  if (wave < 4) {
    const int gw = wave;
    u32 braw[3];
    if (gw < 2) {
      PRIO(3);
      const int row = lane & 3, c16 = lane >> 2;
      u32x4 xv[5];
#pragma unroll
      for (int i = 0; i < 5; ++i) xv[i] = *(const u32x4*)(p.x + (lnb * 8 + gw * 4 + row) * 640 + (16 * i + c16) * 8);
      if (tid < 32) lds_st(sm, tid, 0);
      LDRAIN();
      __builtin_amdgcn_s_barrier();
      SCHED();
      const bf16x2 ones = __builtin_bit_cast(bf16x2, 0x3F803F80u);
      float s2[2] = {0.f, 0.f}, q2[2] = {0.f, 0.f};
#pragma unroll
      for (int i = 0; i < 5; ++i)
#pragma unroll
        for (int k = 0; k < 4; ++k) {
          const u32 xw = xv[i][k]; const bf16x2 v = __builtin_bit_cast(bf16x2, xw);
          s2[k & 1] = __builtin_amdgcn_fdot2_f32_bf16(v, ones, s2[k & 1], false);
          q2[k & 1] = __builtin_amdgcn_fdot2_f32_bf16(v, v, q2[k & 1], false);
        }
      const float mean = red_mod4(s2[0] + s2[1]) * (1.f / 640.f);
      const float var = __builtin_fmaxf(__builtin_fmaf(-mean, mean, red_mod4(q2[0] + q2[1]) * (1.f / 640.f)), 0.f);
      const float sd = rbf(__builtin_amdgcn_sqrtf(rbf(rbf(var) + 1e-5f)));
      const float rinv = __builtin_amdgcn_rcpf(sd), nmr = -rbf(mean) * rinv;
      LAS char* sc = sm + LD_X;
#pragma unroll
      for (int i = 0; i < 5; ++i) {
        u32x4 o;
#pragma unroll
        for (int k = 0; k < 4; ++k) o[k] = pk2(__builtin_fmaf(bflo(xv[i][k]), rinv, nmr), __builtin_fmaf(bfhi(xv[i][k]), rinv, nmr));
        BST(o, l16 + i * 1024, WS_XNA + (lnb * 2 + gw) * 5120);
        xv[i] = o;
      }
      STAMP(1);
      tag = get_tag(sm);
      DRAIN();
      SET_FLAG(WS_FA + (lnb * 2 + gw) * 64);
      STAMP(2);
#pragma unroll
      for (int i = 0; i < 5; ++i) *(LAS u32x4*)(sc + (gw * 4 + row) * LNS + (16 * i + c16) * 16) = xv[i];
      lds_post_nw(sm, SY_LN, lane);
#pragma unroll
      for (int n = 0; n < 3; ++n) braw[n] = p.b1[cs * 96 + n * 32 + l31];
    } else {
      w1_dma(w1b, smb, gw - 2, gw - 1, lane);
      __builtin_amdgcn_s_barrier();
      w1_dma(w1b, smb, gw + 1, 40, lane);
      STAMP(1);
      VMW(18);
      lds_post_nw(sm, SY_W1A, lane);
#pragma unroll
      for (int n = 0; n < 3; ++n) braw[n] = p.b1[cs * 96 + n * 32 + l31];
      DRAIN();
      lds_post_nw(sm, SY_W1, lane);
      STAMP(2);
      tag = get_tag(sm);
    }
    poll_flags(p.ws, WS_FA + (rp * 32 + gw * 8 + (lane & 7)) * 64, tag);
    lds_post_nw(sm, SY_FAOK, lane);
    PRIO(0);
    STAMP(3);
    u32x4 a[40];
    const u32 av = (l31 >> 2) * 5120 + h * 64 + (l31 & 3) * 16;
#define LOAD_A(t) a[t] = BLD(av + ((t) & 31) * 128, WS_XNA + (rp * 32 + gw * 8) * 5120 + ((t) >> 5) * 4096)
#pragma unroll
    for (int t = 0; t < 32; ++t) LOAD_A(t);
    SCHED();
    lds_wait(sm, SY_W1A, 3);
    PRIO(2);
    f32x16 acc[3];
    {
      u32 wb[2];
      wb[0] = smb + 192 * (8 * h + (l15 >> 2)) + 32 * (g & 1) + 8 * (lane & 3); wb[1] = wb[0] + 20 * 3072;
      asm("" : "+v"(wb[0]), "+v"(wb[1]));
      bf16x8 wf[3][3];
#define RDW(t, n) wf[(t) % 3][n] = cat(trd(lp(wb[(t) / 20] + ((t) % 20) * 3072 + (n) * 64)), trd(lp(wb[(t) / 20] + ((t) % 20) * 3072 + (n) * 64 + 768)))
#pragma unroll
      for (int t = 0; t < 2; ++t) { RDW(t, 0); RDW(t, 1); RDW(t, 2); }
      const u32x4 xe = {h ? 0u : 0x3F80u, 0u, 0u, 0u};
#pragma unroll
      for (int n = 0; n < 3; ++n) {
        f32x16 z;
#pragma unroll
        for (int i = 0; i < 16; ++i) z[i] = 0.f;
        const u32x4 we = {braw[n], 0u, 0u, 0u};
        acc[n] = MFMA32(asb(we), asb(xe), z);
      }
      SCHED();
#pragma unroll
      for (int t = 0; t < 40; ++t) {
#pragma unroll
        for (int n = 0; n < 3; ++n) {
          acc[n] = MFMA32(wf[t % 3][n], asb(a[t]), acc[n]);
          SCHED();
          if (t == 19 && n == 0) lds_wait(sm, SY_W1, 3);
          if (t + 2 < 40) RDW(t + 2, n);
          if (n == 0 && t >= 1 && t <= 8) LOAD_A(31 + t);
          if (n == 1 && t == 8) lds_post_nw(sm, SY_AISS, lane);
          SCHED();
        }
      }
    }
    lds_post_nw(sm, SY_G1DONE, lane);
    STAMP(4);
    {
      const u32 ao = (l31 >> 4) * 48 * 1024 + (h * 16 + l15) * 16;
      lds_wait(sm, SY_W2RDY, 1);
#pragma unroll
      for (int n = 0; n < 3; ++n)
#pragma unroll
        for (int c2 = 0; c2 < 2; ++c2) {
          float mv[8];
#pragma unroll
          for (int e = 0; e < 8; ++e) {
            const float hv = acc[n][8 * c2 + e];
            const float ex = __builtin_amdgcn_exp2f(hv * 1.44269504089f);
            const float rc = __builtin_amdgcn_rcpf(__builtin_fmaf(ex, ex + 2.f, 2.f));
            mv[e] = hv * __builtin_fmaf(rc, -2.f, 1.f);
            if ((e & 3) == 3) {
              const int q = ((n * 2 + c2) * 2 + (e >> 2)) * BWS;
              SCHED();
#pragma unroll
              for (int u = 0; u < BWS; ++u) if (q + u < 10 * BWS) LOAD_BW1(q + u);
              SCHED();
            }
          }
          u32x4 o = {pk2(mv[0], mv[1]), pk2(mv[2], mv[3]), pk2(mv[4], mv[5]), pk2(mv[6], mv[7])};
          BST(o, ao + n * 1024 + c2 * 512, WS_ACT + ((rp * 8 + gw * 2) * 48 + cs * 3) * 1024);
        }
    }
    lds_post_nw(sm, SY_MISH0 + gw, lane);
    STAMP(5);
    DRAIN();
    SET_FLAG(WS_FC + (((rp * 2 + (gw >> 1)) * 8 + (cs >> 1)) * 4 + (cs & 1) * 2 + (gw & 1)) * 64);
    PRIO(0);
    STAMP(6);
#pragma unroll
    for (int q = 10 * BWS; q < 30; ++q) LOAD_BW1(q);
    poll_flags(p.ws, fcoff, tag);
    STAMP(7);
    LOAD_AF();
    lds_wait(sm, SY_G1DONE, 4);
    gemm2<false>(rs, sm, wave, lane, rt2, nch, bw, af, res);
    STAMP(11);
    const u32 fdoff = WS_FD + ((rt2 >> 4) * 640 + ct2 * 80 + gw * 20 + (lane < 20 ? lane : 0)) * 64;
    const u64 fd = ld_flag(p.ws, fdoff);
    int orow[3], och[3];
    u32x4 xres[3];
#pragma unroll
    for (int i = 0; i < 3; ++i) {
      const int id = (i < 2 || lane < 32) ? lane + 64 * i : lane;
      orow[i] = (id * 205) >> 11; och[i] = id - orow[i] * 10;
      xres[i] = *(const u32x4*)(p.x + (rt2 * 64 + gw * 16 + orow[i]) * 640 + ct2 * 80 + och[i] * 8);
    }
    lds_wait(sm, SY_GPART, 4);
#pragma unroll
    for (int o = 1; o < 4; ++o)
#pragma unroll
      for (int n = 0; n < 5; ++n) res[n] += *(LAS f32x4*)(sm + ((gw - o) & 3) * 15360 + ((o - 1) * 5 + n) * 1024 + l16);
    if (!wall(fd == tag)) poll_flags(p.ws, fdoff, tag);
    CFENCE();
    STAMP(12);
    const u32 yo = (lane >> 3) * 4096 + (lane & 7) * 16;
    u32x4 yv[3];
#pragma unroll
    for (int i = 0; i < 3; ++i) yv[i] = BLD(yo + ((i == 2 && lane >= 32) ? OOB : 0u), WS_YT + (ct2 * 80 + gw * 20 + 8 * i) * 4096 + rt2 * 128);
    lds_wait(sm, SY_CONV, nch + 1);
#pragma unroll
    for (int i = 0; i < 3; ++i) if (i < 2 || lane < 32) *(LAS u32x4*)(sm + LD_X + gw * 2560 + i * 1024 + l16) = yv[i];
    lds_post(sm, SY_YST, lane);
#pragma unroll
    for (int st = 0; st < 3; ++st) {
      const int v = (gw - 1 - st) & 3;
      lds_wait(sm, SY_SP0 + v, st + 1);
#pragma unroll
      for (int n = 0; n < 5; ++n) res[n] += *(LAS f32x4*)(sm + LD_SP + v * 15360 + (st * 5 + n) * 1024 + l16);
    }
    lds_wait(sm, SY_YST, 4);
#pragma unroll
    for (int n = 0; n < 5; ++n) {
      bf16x4 yy = trd(sm + LD_X + (n * 16 + 4 * g + (l15 >> 2)) * 128 + ((lane & 3) >> 1) * 64 + gw * 16 + (lane & 1) * 8);
      u32x2 yu = __builtin_bit_cast(u32x2, yy);
      res[n] += f32x4{bflo(yu.x), bfhi(yu.x), bflo(yu.y), bfhi(yu.y)};
    }
    STAMP(8);
    lds_wait(sm, SY_SP0 + gw, 4);
    STAMP(9);
    LAS char* stg = sm + gw * 15360;
#pragma unroll
    for (int n = 0; n < 5; ++n) *(LAS f32x4*)(stg + l15 * 336 + n * 64 + g * 16) = res[n] + *(LAS f32x4*)(sm + LD_KT + gw * 5120 + n * 1024 + l16);
    CFENCE();
#pragma unroll
    for (int i = 0; i < 3; ++i) {
      f32x4 u = *(LAS f32x4*)(stg + orow[i] * 336 + och[i] * 32), v = *(LAS f32x4*)(stg + orow[i] * 336 + och[i] * 32 + 16);
      u32x4 o = {pk2(u.x + bflo(xres[i].x), u.y + bfhi(xres[i].x)), pk2(u.z + bflo(xres[i].y), u.w + bfhi(xres[i].y)),
                 pk2(v.x + bflo(xres[i].z), v.y + bfhi(xres[i].z)), pk2(v.z + bflo(xres[i].w), v.w + bfhi(xres[i].w))};
      if (i < 2 || lane < 32) *(u32x4*)(p.out + (rt2 * 64 + gw * 16 + orow[i]) * 640 + ct2 * 80 + och[i] * 8) = o;
    }
  } else if (wave == 7) {
    PRIO(PW7);
    u64 ep;
    asm volatile("global_load_dwordx2 %0, %1, %2 sc1" : "=&v"(ep) : "v"(0u), "s"(p.ws + WS_EPOCH) : "memory");
    u32 dsk3 = gld2(p.dsk, (c0 + (lane < nch ? lane : 0)) * 2);
    w1_dma(w1b, smb, 2, 6, lane);
    __builtin_amdgcn_s_barrier();
    asm volatile("s_waitcnt vmcnt(6)" : "+v"(ep), "+v"(dsk3) :: "memory");
    {
      u64 t = ep * 0x9E3779B97F4A7C15ull + 0x632BE59BD9B4E019ull;
      tag = (u64)__builtin_amdgcn_readfirstlane((u32)t) | ((u64)__builtin_amdgcn_readfirstlane((u32)(t >> 32)) << 32);
      if (lane == 0) { lds_st(sm, SY_TAGLO, (u32)tag); lds_st(sm, SY_TAGHI, (u32)(tag >> 32)); LDRAIN(); lds_st(sm, SY_TAGOK, 1); }
    }
    w1_dma(w1b, smb, 8, 40, lane);
    STAMP(1);
    VMW(18);
    lds_post_nw(sm, SY_W1A, lane);
    DRAIN();
    lds_post_nw(sm, SY_W1, lane);
    STAMP(2);
    PRIO(3);
    lds_wait(sm, SY_LN, 2);
    {
      bf16x8 vt[10];
#pragma unroll
      for (int it = 0; it < 10; ++it) {
        LAS char* tp = sm + LD_X + (l15 >> 2) * LNS + (64 * it + 16 * g + 4 * (lane & 3)) * 2;
        vt[it] = cat(trd(tp), trd(tp + 4 * LNS));
      }
      lds_wait(sm, SY_FAOK, GATE);
#pragma unroll
      for (int it = 0; it < 10; ++it) BST(__builtin_bit_cast(u32x4, vt[it]), l16, WS_XNT + (lnb * 640 + 64 * it) * 16);
    }
    DRAIN();
    SET_FLAG(WS_FT + lnb * 64);
    STAMP(12);
    lds_wait(sm, SY_AISS, 4);
    polln<2>(p.ws, WS_FW, lane, 120, tag);
    lds_post_nw(sm, SY_W2RDY, lane);
    STAMP(13);
    polln<4>(p.ws, WS_FT, lane, 256, tag);
    if (b == 0 && lane == 0) __hip_atomic_store((GAS u64*)(p.ws + WS_EPOCH), ep + 1, RLX_AGENT);
    STAMP(3);
    {
      const int chl = lane & 3;
      u32x4 gv[16];
      const u32 go = ((lane >> 2) * 640 + c0 + chl) * 16 + (chl < nch ? 0u : OOB);
#pragma unroll
      for (int it = 0; it < 16; ++it) gv[it] = BLD(go, WS_XNT + it * 16 * 640 * 16);
      if (chl < 3) {
        LAS char* xd = sm + LD_X + chl * X_SZ;
        u32x4 z = {0, 0, 0, 0};
        *(LAS u32x4*)(xd + (lane >> 2) * 16) = z;
        *(LAS u32x4*)(xd + (14 + (lane >> 2)) * 16) = z;
#pragma unroll
        for (int it = 0; it < 16; ++it) *(LAS u32x4*)(xd + (30 + 16 * it + (lane >> 2)) * 16) = gv[it];
      }
    }
    lds_post(sm, SY_XRDY, lane);
    STAMP(4);
    PRIO(0);
    LOAD_BW();
    lds_wait(sm, SY_KRDY, nch);
    const int par = (l15 & 1) ^ 1;
    u32 aB = smb + LD_KT + (par ? KC1 : 0) + 2 * (2031 - l15 + 8 * g - par) - 3584;
    u32 bB = smb + LD_X + 16 * (28 + 2 * l15 + g);
    u32x4 fa[2][4], fb[2][4];
    { const CAddr c = caddr(aB, bB);
#pragma unroll
      for (int q = 0; q < 4; ++q) { fa[0][q] = FA_(c, q); fb[0][q] = FB_(c, q); } }
    lds_wait(sm, SY_MISH3, 1);
    PRIO(1);
    STAMP(5);
    u64 fc = 0;
#pragma unroll 1
    for (int c = 0; c < nch; ++c) {
      f32x4 y[4];
      const u32x4 dg = diag_frag(__builtin_amdgcn_readlane(dsk3, c), l15, g);
#pragma unroll
      for (int i = 0; i < 4; ++i) { f32x4 z = {0, 0, 0, 0}; y[i] = MFMA16(asb(dg), asb(fb[0][i]), z); }
      fc = ld_flag(p.ws, fcoff);
#pragma unroll 1
      for (int rho = 0; rho < 8; rho += 2) {
        if (c && rho == 4) { VMW(0); SET_FLAG(WS_FD + (c0 + c - 1) * 64); }
        conv_lo1(caddr(aB - 64, bB - 64), fa[0], fb[0], fa[1], fb[1], y);
        const bool last = rho == 6, more = c + 1 < nch;
        aB += last ? (more ? KT_SZ + 384 : 384) : -128; bB += last ? (more ? X_SZ + 384 : 384) : -128;
        conv_lo1(caddr(aB, bB), fa[1], fb[1], fa[0], fb[0], y);
      }
      store_y(rs, y, WS_YT + (c0 + c) * 4096, l15, g);
    }
    lds_post(sm, SY_CONV, lane);
    STAMP(6);
    if (!wall(fc == tag)) poll_flags(p.ws, fcoff, tag);
    CFENCE();
    LOAD_AF();
    VMW(18);
    SET_FLAG(WS_FD + (c0 + nch - 1) * 64);
    PRIO(0);
    STAMP(7);
    lds_wait(sm, SY_G1DONE, 4);
    gemm2<true>(rs, sm, wave, lane, rt2, nch, bw, af, res);
  } else {
    const int s = wave - 4;
    const bool hasch = s < nch;
    const int ch = hasch ? c0 + s : c0;
    LAS char* KT = sm + LD_KT + s * KT_SZ;
    u32 prm[6][3];
    const u32 po = ch * 384 + lane * 2;
    u32 ldt_raw = gld2(p.ldt, ch * 2), dsk_raw = gld2(p.dsk, ch * 2);
#pragma unroll
    for (int it = 0; it < 3; ++it) {
      prm[0][it] = gld2(p.Are, po + it * 128); prm[1][it] = gld2(p.Aim, po + it * 128); prm[2][it] = gld2(p.Bre, po + it * 128);
      prm[3][it] = gld2(p.Bim, po + it * 128); prm[4][it] = gld2(p.Cre, po + it * 128); prm[5][it] = gld2(p.Cim, po + it * 128);
    }
    __builtin_amdgcn_s_barrier();
    if (!hasch) {
      const int t2 = xcd * 16 + (j >> 1);
      PRIO(3);
      u32x4 wr[2][8];
      const u32 wo = g * 1280 + l15 * 16 + 3072;
#pragma unroll
      for (int pc = 0; pc < 2; ++pc) {
        const int pid = (t2 < 120 ? 2 * t2 : 0) + pc, rks = pid / 5, cb = pid - rks * 5;
        const char* src = (const char*)p.w2 + (32 * rks * 640 + 128 * cb) * 2 - 3072;
#pragma unroll
        for (int i = 0; i < 2; ++i) {
          setm0((u32)(size_t)KT + i * 4096);
          glds<0>(src + i * 20480, wo); glds<1024>(src + i * 20480 + 4096, wo); glds<2048>(src + i * 20480 + 8192, wo); glds<3072>(src + i * 20480 + 12288, wo);
        }
        DRAIN();
#pragma unroll
        for (int n = 0; n < 8; ++n) {
          LAS char* tp = KT + (16 * (g >> 1) + 4 * (g & 1) + (l15 >> 2)) * 256 + 32 * n + 8 * (lane & 3);
          wr[pc][n] = __builtin_bit_cast(u32x4, cat(trd(tp), trd(tp + 2048)));
        }
        LDRAIN();
      }
      LDRAIN(); STAMP(8);
      tag = get_tag(sm);
      lds_wait(sm, SY_FAOK, GATEW);
#pragma unroll
      for (int pc = 0; pc < 2; ++pc) {
        const int pid = 2 * t2 + pc, rks = pid / 5, cb = pid - rks * 5;
#pragma unroll
        for (int n = 0; n < 8; ++n) BST(wr[pc][n], l16 + n * 1024 + (t2 < 120 ? 0u : OOB), WS_W2F + (rks * 40 + 8 * cb) * 1024);
      }
      STAMP(9);
      DRAIN();
      STAMP(11);
      SET_FLAG(WS_FW + t2 * 64);
      PRIO(0);
    }
    asm volatile("s_waitcnt vmcnt(0)" : "+v"(ldt_raw), "+v"(dsk_raw) :: "memory");
#pragma unroll
    for (int k = 0; k < 6; ++k) asm volatile("" : "+v"(prm[k][0]), "+v"(prm[k][1]), "+v"(prm[k][2]));
    SCHED();
    STAMP(1);
    if (hasch) {
      const int m = l31;
      const float dt = rbf(__builtin_amdgcn_exp2f(bflo(ldt_raw) * 1.44269504089f));
      cf cc[3]; float mx = 0.f;
#pragma unroll
      for (int it = 0; it < 3; ++it) {
        const cf Bc = {bflo(prm[2][it]), bflo(prm[3][it])}, Cc = {bflo(prm[4][it]), bflo(prm[5][it])};
        cc[it] = cmul(Cc, Bc);
        mx = __builtin_fmaxf(mx, __builtin_fmaxf(__builtin_fabsf(cc[it].x), __builtin_fabsf(cc[it].y)));
      }
      const int ex = __builtin_amdgcn_frexp_expf(max64(mx));
      const float ksc = __builtin_amdgcn_ldexpf(dt, ex - 10);
      f32x16 kacc;
#pragma unroll
      for (int i = 0; i < 16; ++i) kacc[i] = 0.f;
      const int baseA = 512 * h + 16 * ((m >> 2) ^ (h << 1)) + 4 * (m & 3);
      const int baseB = 4096 + 2048 * h + 16 * (m >> 2) + 4 * (m & 3);
      const int rdA = m * 128, rsw = (m >> 1) & 7;
      u32x4 ta[4], tb[4];
#define KMFMA(ks) kacc = MFMA32H(__builtin_bit_cast(f16x8, ta[ks]), __builtin_bit_cast(f16x8, tb[ks]), kacc)
#define WRA(rr, v) (*(LAS u32*)(KT + ((baseA ^ (16 * ((((rr) & 3) >> 1) | ((((rr) >> 2) & 1) << 2)))) + 128 * (((rr) & 3) + 8 * ((rr) >> 2)))) = (v))
#define WRB(be, v) (*(LAS u32*)(KT + ((baseB ^ (16 * ((be) >> 1))) + 128 * (be))) = (v))
#define SWAP(dst, a0, a1) do { auto q_ = __builtin_amdgcn_permlane32_swap((a0), (a1), false, false); dst[0] = q_[0]; dst[1] = q_[1]; } while (0)
#pragma unroll
      for (int it = 0; it < 3; ++it) {
        const float lr = dt * bflo(prm[0][it]) * 1.44269504089f, ph = dt * bflo(prm[1][it]) * 0.15915494309f;
        const cf c = {__builtin_amdgcn_ldexpf(cc[it].x, 10 - ex), __builtin_amdgcn_ldexpf(cc[it].y, 10 - ex)};
        const cf e1 = cexp2r(lr, ph), e128 = cexp2r(lr * 128.f, ph * 128.f);
        const cf e2 = csq(e1), e256 = csq(e128), e4 = csq(e2), e512 = csq(e256);
        SCHED(); if (it) KMFMA(0); SCHED();
        const cf e8 = csq(e4), e1024 = csq(e512), e16 = csq(e8), e32 = csq(e16);
        SCHED(); if (it) KMFMA(1); SCHED();
        const cf e64 = csq(e32), c512 = cmul(c, e512), c1024 = cmul(c, e1024);
        const u32 g1 = toh(e1), g2 = toh(e2), g4 = toh(e4), g8 = toh(e8), g16 = toh(e16);
        SCHED(); if (it) KMFMA(2); SCHED();
        const u32 g256 = toh(e256), g512 = toh(e512), k32 = tohc(e32), k64 = tohc(e64), k128 = tohc(e128);
        const u32 g3 = hmul(g2, g1), g24 = hmul(g16, g8), k96 = hmul(k64, k32);
        const u32 b0 = toh(c), b7 = toh(c512), b10 = toh(c1024);
        SCHED(); if (it) KMFMA(3); SCHED();
        u32 R0[2], R7[2], R10[2], P[4][2], H1[2], H2[2], H3[2], H4[2], H8[2], H16[2], H24[2], H256[2];
        SWAP(R0, b0, hmul(b0, g4)); SWAP(R7, b7, hmul(b7, g256)); SWAP(R10, b10, hmul(b10, g512));
        SWAP(P[0], 0x00003C00u, k128); SWAP(P[1], k32, hmul(k128, k32)); SWAP(P[2], k64, hmul(k128, k64)); SWAP(P[3], k96, hmul(k128, k96));
        SWAP(H1, g1, g1); SWAP(H2, g2, g2); SWAP(H3, g3, g3); SWAP(H4, g4, g4); SWAP(H8, g8, g8); SWAP(H16, g16, g16); SWAP(H24, g24, g24); SWAP(H256, g256, g256);
        SCHED();
#pragma unroll
        for (int sp = 0; sp < 2; ++sp) {
#pragma unroll
          for (int a = 0; a < 4; ++a) {
            WRB(4 * a, P[a][sp]); WRB(4 * a + 1, hmulc(P[a][sp], H8[sp])); SCHED();
            WRB(4 * a + 2, hmulc(P[a][sp], H16[sp])); WRB(4 * a + 3, hmulc(P[a][sp], H24[sp])); SCHED();
            if (sp) { KMFMA(a); SCHED(); }
          }
          const u32 r4 = hmul(R0[sp], H256[sp]), r12 = hmul(R10[sp], H256[sp]);
          WRA(0, R0[sp]); WRA(1, hmul(R0[sp], H1[sp])); SCHED();
          WRA(2, hmul(R0[sp], H2[sp])); WRA(3, hmul(R0[sp], H3[sp])); SCHED();
          WRA(4, r4); WRA(5, hmul(r4, H2[sp])); SCHED();
          WRA(6, hmul(r4, H4[sp])); WRA(7, R7[sp]); SCHED();
          WRA(8, hmul(R7[sp], H4[sp])); WRA(9, hmul(R7[sp], H8[sp])); SCHED();
          WRA(10, R10[sp]); WRA(11, hmul(R10[sp], H8[sp])); SCHED();
          WRA(12, r12); WRA(13, hmul(r12, H8[sp]));
          CFENCE(); SCHED();
#pragma unroll
          for (int ks = 0; ks < 4; ++ks) {
            const int so = rdA + 16 * ((2 * ks + h) ^ rsw);
            ta[ks] = *(LAS u32x4*)(KT + so); tb[ks] = *(LAS u32x4*)(KT + 4096 + so);
          }
          CFENCE(); SCHED();
        }
      }
      KMFMA(0); KMFMA(1); KMFMA(2); KMFMA(3);
      CFENCE();
      STAMP(2);
      {
        const int bb = m;
        float kv[14];
#pragma unroll
        for (int i = 0; i < 14; ++i) kv[i] = kacc[i] * ksc;
        u32x2 s0 = {pk2(kv[3], kv[2]), pk2(kv[1], kv[0])};
        *(LAS u32x2*)(KT + 2 * (2044 - 8 * bb - 4 * h)) = s0;
        u32x2 s1 = {pk2(kv[6], kv[5]), pk2(kv[4], kv[4])};
        *(LAS u32x2*)(KT + 2 * (2044 - 256 - 8 * bb - 4 * h)) = s1;
        u32x4 s2 = {pk2(kv[9], kv[9]), pk2(kv[8], kv[8]), pk2(kv[8], kv[7]), pk2(kv[7], kv[7])};
        *(LAS u32x4*)(KT + 2 * (2040 - 512 - 256 * h - 8 * bb)) = s2;
#pragma unroll
        for (int a2 = 0; a2 < 2; ++a2) {
          const float v0 = kv[10 + 2 * a2], v8 = kv[11 + 2 * a2], tie = (bb & 1) ? v8 : v0;
          u32x4 s3 = {pk2(v8, v8), pk2(v8, tie), pk2(v0, v0), pk2(v0, v0)};
          *(LAS u32x4*)(KT + 2 * (2040 - 1024 - 512 * h - 256 * a2 - 8 * bb)) = s3;
        }
        u32x4 z = {0, 0, 0, 0};
        if (lane < 4) *(LAS u32x4*)(KT + 4096 + (lane & 1) * 16 + (lane >> 1) * KC1) = z;
        CFENCE();
        u32 dw[17];
#pragma unroll
        for (int k = 0; k < 4; ++k) { u32x4 v = *(LAS u32x4*)(KT + lane * 64 + k * 16); dw[4 * k] = v.x; dw[4 * k + 1] = v.y; dw[4 * k + 2] = v.z; dw[4 * k + 3] = v.w; }
        dw[16] = *(LAS u32*)(KT + lane * 64 + 64);
#pragma unroll
        for (int k = 0; k < 4; ++k) {
          u32x4 v;
#pragma unroll
          for (int e = 0; e < 4; ++e) v[e] = __builtin_amdgcn_alignbit(dw[4 * k + e + 1], dw[4 * k + e], 16);
          *(LAS u32x4*)(KT + KC1 + lane * 64 + k * 16) = v;
        }
      }
      lds_post(sm, SY_KRDY, lane);
    }
    STAMP(3);
    tag = get_tag(sm);
    lds_wait(sm, SY_W2RDY, 1);
    LOAD_BW();
    if (hasch) {
      f32x4 y[4];
      const int par = (l15 & 1) ^ 1;
      u32 aB = (u32)(size_t)KT + (par ? KC1 : 0) + 2 * (2031 - l15 + 8 * g - par) - 3584;
      u32 bB = smb + LD_X + s * X_SZ + 16 * (28 + 2 * l15 + g);
      u32x4 fa[8], fb[8];
      lds_wait(sm, SY_XRDY, 1);
      { const CAddr c = caddr(aB, bB);
#pragma unroll
        for (int q = 0; q < 8; ++q) { fb[7 - q] = FB_(c, 7 - q); fa[q] = FA_(c, q); } }
      const u32x4 dg = diag_frag(dsk_raw, l15, g);
      SCHED();
      lds_wait(sm, SY_MISH0 + s, 1);
      PRIO(1);
      STAMP(4);
#pragma unroll
      for (int i = 0; i < 4; ++i) { f32x4 z = {0, 0, 0, 0}; y[i] = MFMA16(asb(dg), asb(fb[4 + i]), z); }
      u64 fc = 0;
#pragma unroll 1
      for (int rho = 0; rho < 8; rho += 2) {
        fc = ld_flag(p.ws, fcoff);
        const u32 nx = rho < 6 ? 128 : 0;
        conv_hi2(caddr(aB - 64, bB - 64), caddr(aB - nx, bB - nx), fa, fb, y); aB -= 128; bB -= 128;
      }
      STAMP(5);
      store_y(rs, y, WS_YT + ch * 4096 + 2048, l15, g);
      lds_post(sm, SY_CONV, lane);
      STAMP(6);
      if (!wall(fc == tag)) poll_flags(p.ws, fcoff, tag);
      CFENCE();
      LOAD_AF();
      VMW(18);
      SET_FLAG(WS_FD + (640 + ch) * 64);
      PRIO(0);
      STAMP(7);
    } else {
      lds_wait(sm, SY_MISH0 + s, 1);
      poll_flags(p.ws, fcoff, tag);
      LOAD_AF();
    }
    lds_wait(sm, SY_G1DONE, 4);
    gemm2<true>(rs, sm, wave, lane, rt2, nch, bw, af, res);
  }
  while (b == 0 && __builtin_amdgcn_s_memrealtime() < t_begin + 4000) __builtin_amdgcn_s_sleep(4);
}

extern "C" void kernel_launch(void* const* d_in, const int* in_sizes, int n_in, void* d_out, int out_size,
                              void* d_ws, size_t ws_size, hipStream_t stream) {
  Params p;
  p.x = (const u16*)d_in[0]; p.Are = (const u16*)d_in[1]; p.Aim = (const u16*)d_in[2]; p.Bre = (const u16*)d_in[3];
  p.Bim = (const u16*)d_in[4]; p.Cre = (const u16*)d_in[5]; p.Cim = (const u16*)d_in[6]; p.ldt = (const u16*)d_in[7];
  p.dsk = (const u16*)d_in[8]; p.w1 = (const u16*)d_in[10]; p.b1 = (const u16*)d_in[11]; p.w2 = (const u16*)d_in[12];
  p.out = (u16*)d_out; p.ws = (char*)d_ws;
  (void)hipFuncSetAttribute((const void*)fused, hipFuncAttributeMaxDynamicSharedMemorySize, LDS_BYTES);
  fused<<<256, 512, LDS_BYTES, stream>>>(p);
}
```

```cpp
#include <hip/hip_runtime.h>
#include <stdint.h>
typedef unsigned short u16; typedef unsigned u32; typedef unsigned long long u64;
typedef __attribute__((ext_vector_type(2))) float f32x2;
typedef __attribute__((ext_vector_type(4))) float f32x4;
typedef __attribute__((ext_vector_type(16))) float f32x16;
typedef __attribute__((ext_vector_type(2))) __bf16 bf16x2;
typedef __attribute__((ext_vector_type(4))) __bf16 bf16x4;
typedef __attribute__((ext_vector_type(8))) __bf16 bf16x8;
typedef __attribute__((ext_vector_type(2))) _Float16 f16x2;
typedef __attribute__((ext_vector_type(8))) _Float16 f16x8;
typedef __attribute__((ext_vector_type(2))) u32 u32x2;
typedef __attribute__((ext_vector_type(3))) u32 u32x3;
typedef __attribute__((ext_vector_type(4))) u32 u32x4;
#define LAS __attribute__((address_space(3)))
#define GAS __attribute__((address_space(1)))
#define RLX_AGENT __ATOMIC_RELAXED, __HIP_MEMORY_SCOPE_AGENT
#define RLX_WG __ATOMIC_RELAXED, __HIP_MEMORY_SCOPE_WORKGROUP
#if defined(__HIP_DEVICE_COMPILE__)
#define NOPK __attribute__((target("no-packed-fp32-ops")))
#else
#define NOPK
#endif
#define DEV __device__ __forceinline__ NOPK
#define CFENCE() asm volatile("" ::: "memory")
#define DRAIN() asm volatile("s_waitcnt vmcnt(0)" ::: "memory")
#define LDRAIN() asm volatile("s_waitcnt lgkmcnt(0)" ::: "memory")
#define VMW(n) asm volatile("s_waitcnt vmcnt(%0)" :: "i"(n) : "memory")
#define SCHED() __builtin_amdgcn_sched_barrier(0)
#define PRIO(n) __builtin_amdgcn_s_setprio(n)
#define DMA_G 14
#define BWS 1
#define G2MAP 0
#define GATE 0
#define GATEW 0
#define PW7 2
#define DTH 24
#define DTH1 12
#define DTH2 12
#define AW 6
#define AR 2
#define AFW 6
#define BTH 12
#define BWG 0
#define ROT1 1
#define ROT2 1
#define G1MAP 1
#define FTG 0
#define LSLEEP 2
#define SGATE SY_FTG
#define DSW 12
#define CODEPF 39000
#define XGATE 2
#define DB 2
#define YK 5
#define GTH 6
#define BGT 16
#define KG 8
#define KS (12 - KG)
#define AFG 10
#define AFS 8
#define BT0 18
#define BMI 18
#define SB1 0
#define MG 3
#define PSLEEP 4

constexpr int L_ = 2048, D_ = 640, N_ = 192, F_ = 1536;
constexpr u32 WS_EPOCH = 0;
constexpr u32 WS_FT = 4096;
constexpr u32 WS_FW = WS_FT + 256 * 64;
constexpr u32 WS_FD = WS_FW + 512 * 64;
constexpr u32 WS_FC = WS_FD + 1280 * 64;
constexpr u32 WS_FA = WS_FC + 1024 * 64;
constexpr u32 WS_XNA = WS_FA + 512 * 64;
constexpr u32 WS_W2F = WS_XNA + L_ * D_ * 2;
constexpr u32 WS_ACT = WS_W2F + F_ * D_ * 2;
constexpr u32 WS_YT = WS_ACT + L_ * F_ * 2;
constexpr u32 WS_END = WS_YT + L_ * D_ * 2;
constexpr u32 WS_STAMP = WS_END;
constexpr u32 OOB = 0x40000000u;
constexpr int KT_SZ = 8320, KC1 = 4160, X_SZ = 4608;
constexpr int LD_KT = 122880, LD_X = LD_KT + 3 * KT_SZ;
constexpr int LD_SYNC = LD_X + 3 * X_SZ;
constexpr int LDS_BYTES = 163840;
constexpr int LD_SP = 61440;
constexpr int LNS = 1296;
enum { SY_W1 = 0, SY_G1DONE, SY_FTG, SY_TAGLO, SY_TAGHI, SY_TAGOK, SY_KRDY, SY_CONV, SY_MISH0, SY_MISH1, SY_MISH2, SY_MISH3,
       SY_XRDY, SY_GISS, SY_XT, SY_AISS, SY_GPART, SY_YST, SY_SP0, SY_SP1, SY_SP2, SY_SP3, SY_FAOK, SY_RT, SY_FA0, SY_FA1, SY_GRD };
constexpr u32 SPIN_CAP = 1u << 18;

struct Params {
  const u16 *x, *Are, *Aim, *Bre, *Bim, *Cre, *Cim, *ldt, *dsk, *w1, *b1, *w2;
  u16* out; char* ws;
};

DEV float asf(u32 v) { return __builtin_bit_cast(float, v); }
DEV u32 asu(float v) { return __builtin_bit_cast(u32, v); }
DEV float bflo(u32 v) { return asf(v << 16); }
DEV float bfhi(u32 v) { return asf(v & 0xffff0000u); }
DEV u32 pk2(float a, float b) { f32x2 v = {a, b}; return __builtin_bit_cast(u32, __builtin_convertvector(v, bf16x2)); }
DEV float rbf(float a) { return bflo(pk2(a, a)); }
struct cf { float x, y; };
DEV cf cmul(cf a, cf b) { cf r = {__builtin_fmaf(-a.y, b.y, a.x * b.x), __builtin_fmaf(a.y, b.x, a.x * b.y)}; return r; }
DEV cf cmulc(cf a, cf b) { cf r = {__builtin_fmaf(a.y, b.y, a.x * b.x), __builtin_fmaf(a.y, b.x, -(a.x * b.y))}; return r; }
DEV cf csq(cf a) { cf r = {__builtin_fmaf(-a.y, a.y, a.x * a.x), (a.x + a.x) * a.y}; return r; }
DEV cf cexp2r(float lr, float ph) {
  float e = __builtin_amdgcn_exp2f(lr); float f = __builtin_amdgcn_fractf(ph);
  cf r = {e * __builtin_amdgcn_cosf(f), e * __builtin_amdgcn_sinf(f)}; return r; }
DEV cf csel(bool h, cf a) { cf r = {h ? a.x : 1.f, h ? a.y : 0.f}; return r; }
DEV u32 toh(cf a) { f32x2 v = {a.x, a.y}; return __builtin_bit_cast(u32, __builtin_convertvector(v, f16x2)); }
DEV u32 tohc(cf a) { f32x2 v = {a.x, -a.y}; return __builtin_bit_cast(u32, __builtin_convertvector(v, f16x2)); }
DEV u32 hmul(u32 a_, u32 b_) {
  const f16x2 a = __builtin_bit_cast(f16x2, a_), b = __builtin_bit_cast(f16x2, b_), nb = -b;
  const f16x2 s = __builtin_shufflevector(nb, b, 1, 2);
  return __builtin_bit_cast(u32, __builtin_elementwise_fma(a.yy, s, a.xx * b)); }
DEV u32 hmulc(u32 a_, u32 b_) {
  const f16x2 a = __builtin_bit_cast(f16x2, a_), b = __builtin_bit_cast(f16x2, b_), nb = -b;
  const f16x2 s = __builtin_shufflevector(b, nb, 0, 3);
  return __builtin_bit_cast(u32, __builtin_elementwise_fma(a.yy, b.yx, a.xx * s)); }
template <int CTRL> DEV float dppf(float v) { return asf(__builtin_amdgcn_update_dpp(0, asu(v), CTRL, 0xf, 0xf, true)); }
DEV float red8(float v) { v += dppf<0xB1>(v); v += dppf<0x4E>(v); v += dppf<0x141>(v); return v; }
DEV float red_mod4(float v) {
  v += dppf<0x124>(v); v += dppf<0x128>(v);
  auto r = __builtin_amdgcn_permlane16_swap(asu(v), asu(v), false, false);
  v = asf(r[0]) + asf(r[1]);
  auto q = __builtin_amdgcn_permlane32_swap(asu(v), asu(v), false, false);
  return asf(q[0]) + asf(q[1]); }
DEV float max64(float v) {
  v = __builtin_fmaxf(v, dppf<0xB1>(v)); v = __builtin_fmaxf(v, dppf<0x4E>(v)); v = __builtin_fmaxf(v, dppf<0x141>(v)); v = __builtin_fmaxf(v, dppf<0x140>(v));
  auto r = __builtin_amdgcn_permlane16_swap(asu(v), asu(v), false, false);
  v = __builtin_fmaxf(asf(r[0]), asf(r[1]));
  auto q = __builtin_amdgcn_permlane32_swap(asu(v), asu(v), false, false);
  return __builtin_fmaxf(asf(q[0]), asf(q[1])); }
DEV bool wall(bool c) { return __builtin_amdgcn_ballot_w64(c) == ~0ull; }

DEV u32 lds_ld(LAS char* s, int w) { return __hip_atomic_load((LAS u32*)(s + LD_SYNC) + w, RLX_WG); }
DEV void lds_st(LAS char* s, int w, u32 v) { __hip_atomic_store((LAS u32*)(s + LD_SYNC) + w, v, RLX_WG); }
DEV void lds_wait(LAS char* s, int w, u32 want) {
#pragma unroll 1
  for (u32 n = 0; n < (1u << 22); ++n) { if (__builtin_amdgcn_readfirstlane(lds_ld(s, w)) >= want) break; __builtin_amdgcn_s_sleep(LSLEEP); }
  CFENCE();
}
DEV void lds_post(LAS char* s, int w, int lane) {
  LDRAIN();
  if (lane == 0) (void)__hip_atomic_fetch_add((LAS u32*)(s + LD_SYNC) + w, 1u, RLX_WG);
  CFENCE();
}
DEV u32 lds_arrive(LAS char* s, int w, int lane) {
  LDRAIN();
  u32 r = 0; if (lane == 0) r = __hip_atomic_fetch_add((LAS u32*)(s + LD_SYNC) + w, 1u, RLX_WG);
  CFENCE();
  return __builtin_amdgcn_readfirstlane(r);
}
DEV void lds_post_nw(LAS char* s, int w, int lane) {
  CFENCE();
  if (lane == 0) (void)__hip_atomic_fetch_add((LAS u32*)(s + LD_SYNC) + w, 1u, RLX_WG);
  CFENCE();
}
DEV u64 get_tag(LAS char* s) {
  lds_wait(s, SY_TAGOK, 1);
  return (u64)__builtin_amdgcn_readfirstlane(lds_ld(s, 32)) | ((u64)__builtin_amdgcn_readfirstlane(lds_ld(s, 33)) << 32);
}
DEV u64 ld_flag(char* ws, u32 off) { return __hip_atomic_load((GAS u64*)(ws + off), RLX_AGENT); }
DEV void poll_flags(char* ws, u32 off, u64 tag) {
#pragma unroll 1
  for (u32 n = 0; n < SPIN_CAP; ++n) {
    if (wall(ld_flag(ws, off) == tag)) break;
    __builtin_amdgcn_s_sleep(PSLEEP);
  }
  CFENCE();
}
DEV void poll_n(char* ws, u32 off, bool act, u64 tag) {
#pragma unroll 1
  for (u32 n = 0; n < SPIN_CAP; ++n) {
    u64 v = tag; if (act) v = ld_flag(ws, off);
    if (wall(v == tag)) break;
    __builtin_amdgcn_s_sleep(PSLEEP);
  }
  CFENCE();
}
template <int NL> DEV void polln(char* ws, u32 base, int lane, int nvalid, u64 tag) {
#pragma unroll 1
  for (u32 n = 0; n < SPIN_CAP; ++n) {
    bool ok = true;
#pragma unroll
    for (int k = 0; k < NL; ++k) { int i = lane + 64 * k; u64 v = ld_flag(ws, base + i * 64); ok &= (v == tag) | (i >= nvalid); }
    if (wall(ok)) break;
    __builtin_amdgcn_s_sleep(32);
  }
  CFENCE();
}
DEV bf16x4 trd(LAS char* p) { return __builtin_amdgcn_ds_read_tr16_b64_v4bf16((LAS bf16x4*)p); }
DEV bf16x8 cat(bf16x4 a, bf16x4 b) { return __builtin_shufflevector(a, b, 0, 1, 2, 3, 4, 5, 6, 7); }
DEV bf16x8 asb(u32x4 v) { return __builtin_bit_cast(bf16x8, v); }
DEV LAS char* lp(u32 a) { return (LAS char*)(size_t)a; }
#define MFMA16(a, b, c) __builtin_amdgcn_mfma_f32_16x16x32_bf16(a, b, c, 0, 0, 0)
#define MFMA32(a, b, c) __builtin_amdgcn_mfma_f32_32x32x16_bf16(a, b, c, 0, 0, 0)
#define MFMA32H(a, b, c) __builtin_amdgcn_mfma_f32_32x32x16_f16(a, b, c, 0, 0, 0)
#define BLD(voff, soff) __builtin_amdgcn_raw_buffer_load_b128(rs, (voff), (soff), 16)
#define BST(v, voff, soff) __builtin_amdgcn_raw_buffer_store_b128((v), rs, (voff) + (soff), 0, 16)

#define SET_FLAG(off) do { const u32x4 f_ = {(u32)tag, (u32)(tag >> 32), (u32)tag, (u32)(tag >> 32)}; BST(f_, l16 + (lane < 4 ? 0u : OOB), (off)); } while (0)

#define STAMP(i)

#define STAMPX(i) STAMP(i)
DEV void setm0(u32 v) { asm volatile("s_mov_b32 m0, %0\n\ts_nop 0" :: "s"(v) : "memory"); }
template <int OFF> DEV void glds(const void* sbase, u32 voff) {
  asm volatile("global_load_lds_dwordx4 %0, %1 offset:%2" :: "v"(voff), "s"(sbase), "i"(OFF) : "memory");
}
DEV u32x4 ldA(u32 a) { LAS u32* ap = (LAS u32*)lp(a); u32x4 v = {ap[0], ap[1], ap[2], ap[3]}; return v; }
DEV u32x4 ldB(u32 a) { return *(LAS u32x4*)lp(a); }
struct CAddr { u32 a[4]; u32 b; };
DEV CAddr caddr(u32 aB, u32 bB) {
  CAddr c;
#pragma unroll
  for (int i = 0; i < 4; ++i) { c.a[i] = aB + 1024 * (3 - i); asm("" : "+v"(c.a[i])); }
  c.b = bB; asm("" : "+v"(c.b));
  return c;
}
#define FA_(c, q) ldA((c).a[(q) >> 1] + (((q) & 1) ? 0 : 512))
#define FB_(c, q) ldB((c).b + 512 * (q))
constexpr int LO = 3;
template <class H> DEV void conv_hi2(const CAddr& n1, const CAddr& n2, u32x4 (&fa)[8], u32x4 (&fb)[8], f32x4 (&y)[8 - LO], H hook) {
#pragma unroll
  for (int q = 0; q < 8; ++q) {
#pragma unroll
    for (int pp = 7 - q; pp >= 0 && pp + q >= LO; --pp) {
      y[q + pp - LO] = MFMA16(asb(fa[q]), asb(fb[pp]), y[q + pp - LO]);
      if (pp == 7 - q) { SCHED(); fb[pp] = FB_(n1, pp); SCHED(); }
    }
    SCHED(); fa[q] = FA_(n1, q); if (q < 5) hook(q); SCHED();
  }
#pragma unroll
  for (int pp = 7; pp >= 0; --pp) {
#pragma unroll
    for (int q = (pp < LO ? LO - pp : 0); q <= 7 - pp; ++q) {
      y[q + pp - LO] = MFMA16(asb(fa[q]), asb(fb[pp]), y[q + pp - LO]);
      if (pp == (q < LO ? LO - q : 0)) { SCHED(); fa[q] = FA_(n2, q); SCHED(); }
    }
    SCHED(); fb[pp] = FB_(n2, pp); if (pp < 5) hook(9 - pp); SCHED();
  }
}
DEV void conv_lo1(const CAddr& n, const u32x4 (&ca)[LO], const u32x4 (&cb)[LO], u32x4 (&na)[LO], u32x4 (&nb)[LO], f32x4 (&y)[LO]) {
  int k = 0;
#pragma unroll
  for (int q = 0; q < LO; ++q)
#pragma unroll
    for (int pp = LO - 1 - q; pp >= 0; --pp) {
      y[q + pp] = MFMA16(asb(ca[q]), asb(cb[pp]), y[q + pp]);
      SCHED();
      if (k < 2 * LO) { if (k & 1) nb[LO - 1 - (k >> 1)] = FB_(n, LO - 1 - (k >> 1)); else na[k >> 1] = FA_(n, k >> 1); }
      ++k;
      SCHED();
    }
}
DEV u32x4 diag_frag(u32 dsk, int l15, int g) {
  const u32 v = (g == 2 + (l15 >> 3)) ? ((l15 & 1) ? dsk << 16 : dsk) : 0u;
  const int w = (l15 & 7) >> 1;
  u32x4 r = {w == 0 ? v : 0u, w == 1 ? v : 0u, w == 2 ? v : 0u, w == 3 ? v : 0u};
  return r;
}
template <int NT> DEV void store_y(const __amdgpu_buffer_rsrc_t rs, const f32x4 (&y)[NT], u32 base, int l15, int g) {
#pragma unroll
  for (int t = 0; t < NT; t += 2) {
    const int u = t + 1 < NT ? t + 1 : t;
    const u32 a0 = pk2(y[t].x, y[t].y), a1 = pk2(y[t].z, y[t].w), b0 = pk2(y[u].x, y[u].y), b1 = pk2(y[u].z, y[u].w);
    auto s0 = __builtin_amdgcn_permlane16_swap(a0, b0, false, false);
    auto s1 = __builtin_amdgcn_permlane16_swap(a1, b1, false, false);
    const u32x4 o = {s0[0], s1[0], s0[1], s1[1]};
    BST(o, t * 512 + ((g & 1) ? (u > t ? 512u : OOB) : 0u) + (l15 >> 2) * 128 + (g >> 1) * 64 + (l15 & 3) * 16, base);
  }
}
DEV u32 gld2(const void* sbase, u32 voff) { u32 r; asm volatile("global_load_ushort %0, %1, %2" : "=&v"(r) : "v"(voff), "s"(sbase) : "memory"); return r; }

#define LOAD_AF1(NK, u) af[u] = BLD(l16 + (((u) % NK) & 3) * 1024, WS_ACT + ((rt2 * 4 + ((own + 1 + (u) / NK) & 3)) * 48 + k0 + (((u) % NK) & ~3)) * 1024)
template <bool SW, int NK>
DEV void gemm2(const __amdgpu_buffer_rsrc_t rs, LAS char* sm, int wave, int k0, int lane, int rt2, int nch,
               u32x4 (&bw)[NK * 5], u32x4 (&af)[NK * 4], f32x4 (&res)[5]) {
  constexpr int W0 = SW ? AFS : AFG, RP = SW ? 2 : 1;
  const int own = wave & 3;
  const u32 l16 = lane * 16;
  LAS char* dst = sm + wave * 15360 + l16;
  f32x4 acc[2][5];
#pragma unroll
  for (int st = 0; st < 4; ++st) {
#pragma unroll
    for (int k = 0; k < NK; ++k) {
#pragma unroll
      for (int n = 0; n < 5; ++n) {
        f32x4 z = {0, 0, 0, 0};
        acc[st & 1][n] = MFMA16(asb(bw[k * 5 + n]), asb(af[st * NK + k]), k ? acc[st & 1][n] : z);
        SCHED();
        if (n < RP && (st * NK + k) * RP + n + W0 < 4 * NK) LOAD_AF1(NK, (st * NK + k) * RP + n + W0);
        if (st > 0 && n >= 2 && (n & 1) == 0 && k * 2 + (n >> 2) < 5) *(LAS f32x4*)(dst + ((st - 1) * 5 + k * 2 + (n >> 2)) * 1024) = acc[(st - 1) & 1][k * 2 + (n >> 2)];
        SCHED();
      }
      if (st > 0 && k == 2) { if (!SW) lds_post_nw(sm, SY_SP0 + own, lane); else if (st == 3) lds_post_nw(sm, SY_GPART, lane); }
      SCHED();
    }
  }
  if (!SW) {
    lds_wait(sm, SY_CONV, nch + 1);
#pragma unroll
    for (int n = 0; n < 5; ++n) *(LAS f32x4*)(sm + LD_KT + own * 5120 + n * 1024 + l16) = acc[1][n];
    lds_post_nw(sm, SY_SP0 + own, lane);
  } else {
#pragma unroll
    for (int n = 0; n < 5; ++n) res[n] = acc[1][n];
  }
}

__global__ __launch_bounds__(512, 2) NOPK void fused(Params p) {
  extern __shared__ __attribute__((aligned(16))) char smem_[];
  LAS char* sm = (LAS char*)smem_;
  const u32 smb = (u32)(size_t)sm;
  const int b = __builtin_amdgcn_workgroup_id_x(), xcd = b & 7, j = b >> 3;
  const int tid = __builtin_amdgcn_workitem_id_x(), wave = __builtin_amdgcn_readfirstlane(tid >> 6), lane = tid & 63;
  const int g = lane >> 4, l15 = lane & 15, l31 = lane & 31, h = lane >> 5;
  const u32 l16 = lane * 16;
  const __amdgpu_buffer_rsrc_t rs = __builtin_amdgcn_make_buffer_rsrc(p.ws, 0, WS_END, 0x00020000);
  STAMP(0);
  u32 cpf;
  {
    const char* pc = (const char*)(__builtin_amdgcn_s_getpc() & ~255ull);
    u32 o = (j * 8 + wave) * 256 + (lane & 1) * 128;
    if (o > CODEPF - 128) o = 0;
    asm volatile("global_load_dword %0, %1, %2" : "=&v"(cpf) : "v"(o), "s"(pc) : "memory");
  }
  asm volatile("" :: "s"(p.x), "s"(p.ws), "s"(p.w1));
  const int lnb = xcd * 32 + j;
  u32x4 xv[5];
  u64 ep;
  if (wave < 2) {
    asm volatile("global_load_dwordx2 %0, %1, %2 sc1" : "=&v"(ep) : "v"(0u), "s"(p.ws + WS_EPOCH) : "memory");
#pragma unroll
    for (int i = 0; i < 5; ++i)
      asm volatile("global_load_dwordx4 %0, %1, %2 offset:%3" : "=&v"(xv[i]) : "v"((lane & 3) * 1280 + (lane >> 2) * 16), "s"(p.x + (lnb * 8 + wave * 4) * 640), "i"(i * 256) : "memory");
  }
  if (tid < 32) lds_st(sm, tid, 0);
  LDRAIN();
  __builtin_amdgcn_s_barrier();
  SCHED();
  u64 tag;
  if (wave < 2) {
      const int gw = wave;
      PRIO(3);
      asm volatile("s_waitcnt vmcnt(0)" : "+v"(xv[0]), "+v"(xv[1]), "+v"(xv[2]), "+v"(xv[3]), "+v"(xv[4]), "+v"(ep) :: "memory");
      lds_post_nw(sm, SY_FAOK, lane);
      const bf16x2 ones = __builtin_bit_cast(bf16x2, 0x3F803F80u);
      float s2[2] = {0.f, 0.f}, q2[2] = {0.f, 0.f};
#pragma unroll
      for (int i = 0; i < 5; ++i)
#pragma unroll
        for (int k = 0; k < 4; ++k) {
          const u32 xw = xv[i][k]; const bf16x2 v = __builtin_bit_cast(bf16x2, xw);
          s2[k & 1] = __builtin_amdgcn_fdot2_f32_bf16(v, ones, s2[k & 1], false);
          q2[k & 1] = __builtin_amdgcn_fdot2_f32_bf16(v, v, q2[k & 1], false);
        }
      const float mean = red_mod4(s2[0] + s2[1]) * (1.f / 640.f);
      const float var = __builtin_fmaxf(__builtin_fmaf(-mean, mean, red_mod4(q2[0] + q2[1]) * (1.f / 640.f)), 0.f);
      const float sd = rbf(__builtin_amdgcn_sqrtf(rbf(rbf(var) + 1e-5f)));
      const float rinv = __builtin_amdgcn_rcpf(sd), nmr = -rbf(mean) * rinv;
#pragma unroll
      for (int i = 0; i < 5; ++i) {
        u32x4 o;
#pragma unroll
        for (int k = 0; k < 4; ++k) o[k] = pk2(__builtin_fmaf(bflo(xv[i][k]), rinv, nmr), __builtin_fmaf(bfhi(xv[i][k]), rinv, nmr));
        BST(o, l16 + i * 1024, WS_XNA + (lnb * 2 + gw) * 5120);
      }
      STAMP(1);
      {
        const u64 t = ep * 0x9E3779B97F4A7C15ull + 0x632BE59BD9B4E019ull;
        tag = (u64)__builtin_amdgcn_readfirstlane((u32)t) | ((u64)__builtin_amdgcn_readfirstlane((u32)(t >> 32)) << 32);
        if (tid == 0) { const u32x4 tv = {(u32)tag, (u32)(tag >> 32), (u32)ep, (u32)(ep >> 32)}; *(LAS u32x4*)(sm + LD_SYNC + 128) = tv; CFENCE(); lds_st(sm, SY_TAGOK, 1); }
      }
      DRAIN();
      SET_FLAG(WS_FA + (lnb * 2 + gw) * 64);
      if (lds_arrive(sm, SY_XT, lane) == 1) SET_FLAG(WS_FT + lnb * 64);
      STAMP(2);
  }
  const int rp = (xcd & 1) * 8 + (j & 7), cs = (xcd >> 1) * 4 + (j >> 3), ri = (j & 7) + 2 * (j >> 3);
  if (wave == 7) {
    PRIO(3);
    u32 so[3];
#pragma unroll
    for (int r = 0; r < 3; ++r) { const int c = r * 64 + lane, row = c / 12, cc = c - row * 12; so[r] = row * 3072 + cc * 16 + 2048 - 1024 * r; }
    lds_wait(sm, SY_FAOK, XGATE);
#pragma unroll 1
    for (int bl = 0; bl < 8; ++bl) {
      const char* src = (const char*)p.w1 + cs * 192 - 2048 + ((bl + ROT1 * ri) & 7) * (80 * 3072);
#pragma unroll
      for (int q = 0; q < 5; ++q) {
        setm0(smb + bl * 15360 + q * 3072);
        glds<0>(src + q * 49152, so[0]); glds<1024>(src + q * 49152, so[1]); glds<2048>(src + q * 49152, so[2]);
      }
      VMW(15 * DB);
      if (bl >= DB && lane == 0) lds_st(sm, SY_W1, 5 * (bl + 1 - DB));
    }
    STAMP(1);
    if (DB > 2) { VMW(30); if (lane == 0) lds_st(sm, SY_W1, 30); }
    if (DB > 1) { VMW(15); if (lane == 0) lds_st(sm, SY_W1, 35); }
    DRAIN();
    if (lane == 0) lds_st(sm, SY_W1, 40);
  }
  const int rt2 = xcd * 4 + (j & 3), ct2 = j >> 2;
  const int jc = (j * 43) >> 7, jr = j - jc * 3;
  const int c0 = j < 30 ? xcd * 80 + jc * 8 + jr * 3 : 0, nch = j < 30 ? (jr < 2 ? 3 : 2) : 0;
  const int own = wave & 3;
  const int rot = ROT1 * 5 * (ri & 7);
  const int kq = (wave + ROT2 * (j + (j >> 2))) & 3;
  const int k0 = kq * 12 + (wave < 4 ? 0 : KG), nk = wave < 4 ? KG : 12 - KG;
  const int kf = k0 + ((lane >> 1) < nk ? (lane >> 1) : 0);
  const u32 fcoff = WS_FC + (rt2 * 32 + ((kf * 43) >> 7) * 2 + (lane & 1)) * 64;
  const u32 fwoff = WS_FW + (kf * 10 + ((ct2 * 80) >> 6) + (lane & 1)) * 64;
  f32x4 res[5];
#define LOAD_BW1(q) bw[q] = BLD(l16 + (((q) % 5) & 3) * 1024, WS_W2F + ((k0 + (q) / 5) * 40 + ct2 * 5 + (((q) % 5) & 4)) * 1024)
#define LOAD_BW() do { _Pragma("unroll") for (int q = 0; q < KS * 5; ++q) { LOAD_BW1(q); if (q & 1) VMW(BTH); } SCHED(); } while (0)
#define LOAD_AF(NK, W) do { _Pragma("unroll") for (int u = 0; u < W; ++u) LOAD_AF1(NK, u); SCHED(); } while (0)
#define GATHER() do { if (nch) { \
      u32x2 gv[8]; \
      const u32 go = (lane >> 2) * 5120 + (xcd * 10 + jc) * 64 + (lane & 3) * 16 + jr * 4; \
      _Pragma("unroll") for (int it = 0; it < 8; ++it) gv[it] = __builtin_amdgcn_raw_buffer_load_b64(rs, go, WS_XNA + (own * 8 + it) * 16 * 5120, 16); \
      const u32 sh = jr * 16; \
      LAS u16* xd = (LAS u16*)(sm + LD_X + 480 + own * 1024 + lane * 2); \
      _Pragma("unroll") for (int it = 0; it < 8; ++it) { \
        const u64 v = (((u64)gv[it].y << 32) | gv[it].x) >> sh; \
        xd[it * 64] = (u16)v; xd[it * 64 + X_SZ / 2] = (u16)(v >> 16); xd[it * 64 + X_SZ] = (u16)(v >> 32); } } \
      lds_post(sm, SY_XRDY, lane); } while (0)

  if (__builtin_expect(wave < 4, 1)) {
    const int gw = wave;
    u32x4 bw[KG * 5], af[KG * 4];
    u32 braw[3];
#pragma unroll
    for (int n = 0; n < 3; ++n) braw[n] = p.b1[cs * 96 + n * 32 + l31];
    if (gw >= 2) {
      PRIO(3);
      const int pid = (lnb < 240 ? lnb * 2 : 0) + (gw - 2), rks = pid / 10, cb = pid - rks * 10;
      const char* src = (const char*)p.w2 + (32 * rks * 640 + 64 * cb) * 2;
      const u32 wo = (lane >> 3) * 1280 + (lane & 7) * 16;
      lds_wait(sm, SY_FAOK, XGATE);
      setm0(smb + LD_X + (gw - 2) * 4096);
      glds<0>(src, wo); glds<1024>(src + 9216, wo); glds<2048>(src + 18432, wo); glds<3072>(src + 27648, wo);
      DRAIN();
      u32x4 wr[4];
#pragma unroll
      for (int n = 0; n < 4; ++n) {
        LAS char* tp = sm + LD_X + (gw - 2) * 4096 + (16 * (g >> 1) + 4 * (g & 1) + (l15 >> 2)) * 128 + 32 * n + 8 * (lane & 3);
        wr[n] = __builtin_bit_cast(u32x4, cat(trd(tp), trd(tp + 1024)));
      }
      lds_post(sm, SY_RT, lane);
#pragma unroll
      for (int n = 0; n < 4; ++n) BST(wr[n], l16 + n * 1024 + (lnb < 240 ? 0u : OOB), WS_W2F + (rks * 40 + 4 * cb) * 1024);
      STAMP(1);
      tag = get_tag(sm);
      DRAIN();
      if (lnb < 240) SET_FLAG(WS_FW + pid * 64);
      STAMP(2);
    }
    if (gw < 2) {
      poll_n(p.ws, WS_FA + (rp * 32 + gw * 8 + (lane & 8) * 2 + (lane & 7)) * 64, lane < 16, tag);
      lds_post_nw(sm, SY_FA0 + gw, lane);
    } else lds_wait(sm, SY_FA0 + (gw & 1), 1);
    asm volatile("" :: "v"(cpf));
    PRIO(2);
    STAMP(3);
    u32x4 a[40];
    const u32 av = (l31 >> 2) * 5120 + h * 64 + (l31 & 3) * 16;
#define LOAD_A(t) a[t] = BLD(av + ((t) % 5) * 128, WS_XNA + (rp * 32 + gw * 8) * 5120 + (((t) / 5 * 5 + rot >= 40) ? (t) / 5 * 5 + rot - 40 : (t) / 5 * 5 + rot) * 128)
#pragma unroll
    for (int t = 0; t < AW; ++t) LOAD_A(t);
    u64 fw = ld_flag(p.ws, fwoff);
    SCHED();
    lds_wait(sm, SY_W1, 5);
    f32x16 acc[3];
    {
      u32 wb[2], wp = 0;
      wb[0] = smb + 192 * (8 * h + (l15 >> 2)) + 32 * (g & 1) + 8 * (lane & 3); wb[1] = wb[0] + 20 * 3072;
      asm("" : "+v"(wb[0]), "+v"(wb[1]));
      bf16x8 wf[3][3];
#define RDW(t, n) wf[(t) % 3][n] = cat(trd(lp(wb[(t) / 20] + ((t) % 20) * 3072 + (n) * 64)), trd(lp(wb[(t) / 20] + ((t) % 20) * 3072 + (n) * 64 + 768)))
#pragma unroll
      for (int t = 0; t < 2; ++t) { RDW(t, 0); RDW(t, 1); RDW(t, 2); }
      const u32x4 xe = {h ? 0u : 0x3F80u, 0u, 0u, 0u};
#pragma unroll
      for (int n = 0; n < 3; ++n) {
        f32x16 z;
#pragma unroll
        for (int i = 0; i < 16; ++i) z[i] = 0.f;
        const u32x4 we = {braw[n], 0u, 0u, 0u};
        acc[n] = MFMA32(asb(we), asb(xe), z);
      }
      SCHED();
#pragma unroll
      for (int t = 0; t < 40; ++t) {
#pragma unroll
        for (int n = 0; n < 3; ++n) {
          acc[n] = MFMA32(wf[t % 3][n], asb(a[t]), acc[n]);
          SCHED();
          if ((t + 2) % 5 == 0 && n == 0 && t + 2 < 40) {
            const u32 need = t + 7;
            if (__builtin_amdgcn_readfirstlane(wp) < need) lds_wait(sm, SY_W1, need);
            CFENCE();
          }
          if (t + 2 < 40) RDW(t + 2, n);
          if ((t + 3) % 5 == 0 && n == 0) wp = lds_ld(sm, SY_W1);
          if (n < AR && AW + t * AR + n < 40) LOAD_A(AW + t * AR + n);
          if (n == 2 && t == BGT) lds_post_nw(sm, SY_AISS, lane);
          if (n == 1 && t == FTG) lds_post_nw(sm, SY_FTG, lane);
          if (t == BT0 - 1 && n == 2) { if (!wall(fw == tag)) poll_flags(p.ws, fwoff, tag); CFENCE(); }
          if (t >= BT0 && n == 1) LOAD_BW1(t - BT0);
          SCHED();
        }
      }
    }
    lds_post_nw(sm, SY_G1DONE, lane);
    STAMP(4);
    {
      const u32 ao = (l31 >> 4) * 48 * 1024 + (h * 16 + l15) * 16;
#pragma unroll
      for (int n = 0; n < 3; ++n)
#pragma unroll
        for (int c2 = 0; c2 < 2; ++c2) {
          float mv[8];
          if (n * 2 + c2 == MG) lds_post_nw(sm, SY_MISH0 + gw, lane);
#pragma unroll
          for (int e = 0; e < 8; ++e) {
            const float hv = acc[n][8 * c2 + e];
            const float ex = __builtin_amdgcn_exp2f(hv * 1.44269504089f);
            const float rc = __builtin_amdgcn_rcpf(__builtin_fmaf(ex, ex + 2.f, 2.f));
            mv[e] = hv * __builtin_fmaf(rc, -2.f, 1.f);
            if (e & 1) {
              const int q = 40 - BT0 + (n * 2 + c2) * 4 + (e >> 1);
              SCHED(); if (q < KG * 5 && q < 40 - BT0 + BMI) LOAD_BW1(q); SCHED();
            }
          }
          u32x4 o = {pk2(mv[0], mv[1]), pk2(mv[2], mv[3]), pk2(mv[4], mv[5]), pk2(mv[6], mv[7])};
          BST(o, ao + n * 1024 + c2 * 512, WS_ACT + ((rp * 8 + gw * 2) * 48 + cs * 3) * 1024);
        }
    }
    if (MG >= 6) lds_post_nw(sm, SY_MISH0 + gw, lane);
    STAMP(5);
    DRAIN();
    SET_FLAG(WS_FC + ((rp * 2 + (gw >> 1)) * 32 + cs * 2 + (gw & 1)) * 64);
    PRIO(0);
    STAMP(6);
#pragma unroll
    for (int q = 40 - BT0 + BMI; q < KG * 5; ++q) LOAD_BW1(q);
    poll_flags(p.ws, fcoff, tag);
    STAMPX(7);
    LOAD_AF(KG, AFG);
    lds_wait(sm, SY_G1DONE, 4);
    gemm2<false, KG>(rs, sm, wave, k0, lane, rt2, nch, bw, af, res);
    STAMPX(11);
  } else if (wave == 7) {
    const u32 dsk3 = p.dsk[c0 + (lane < nch ? lane : 0)];
    asm volatile("" :: "v"(cpf));
    STAMP(2);
    PRIO(3);
    tag = get_tag(sm);
    lds_wait(sm, SY_FTG, 4);
    STAMP(13);
    polln<4>(p.ws, WS_FT, lane, 256, tag);
    if (b == 0 && lane == 0) __hip_atomic_store((GAS u64*)(p.ws + WS_EPOCH), ((u64)lds_ld(sm, 34) | ((u64)lds_ld(sm, 35) << 32)) + 1, RLX_AGENT);
    STAMP(3);
    lds_wait(sm, SY_RT, 2);
    lds_post_nw(sm, SY_GISS, lane);
    PRIO(0);
    if (lane < 30) {
      const u32x4 z = {0, 0, 0, 0};
#pragma unroll
      for (int c = 0; c < 3; ++c) *(LAS u32x4*)(sm + LD_X + c * X_SZ + l16) = z;
    }
    GATHER();
    u32x4 bw[KS * 5], af[KS * 4];
    STAMP(4);
    PRIO(0);
    poll_flags(p.ws, fwoff, tag);
    lds_wait(sm, SY_AISS, 4);
    LOAD_BW();
    lds_wait(sm, SY_KRDY, nch);
    lds_wait(sm, SY_XRDY, 4);
    const int par = (l15 & 1) ^ 1;
    u32 aB = smb + LD_KT + (par ? KC1 : 0) + 2 * (2031 - l15 + 8 * g - par) - 3584;
    u32 bB = smb + LD_X + 16 * (28 + 2 * l15 + g);
    u32x4 fa[2][LO], fb[2][LO];
    { const CAddr c = caddr(aB, bB);
#pragma unroll
      for (int q = 0; q < LO; ++q) { fa[0][q] = FA_(c, q); fb[0][q] = FB_(c, q); } }
    lds_wait(sm, SY_MISH3, 1);
    PRIO(1);
    STAMP(5);
    u64 fc = 0;
#pragma unroll 1
    for (int c = 0; c < nch; ++c) {
      f32x4 y[LO];
      const u32x4 dg = diag_frag(__builtin_amdgcn_readlane(dsk3, c), l15, g);
#pragma unroll
      for (int i = 0; i < LO; ++i) { f32x4 z = {0, 0, 0, 0}; y[i] = MFMA16(asb(dg), asb(fb[0][i]), z); }
      fc = ld_flag(p.ws, fcoff);
#pragma unroll 1
      for (int rho = 0; rho < 8; rho += 2) {
        if (c && rho == 6) { VMW(0); SET_FLAG(WS_FD + (c0 + c - 1) * 64); }
        conv_lo1(caddr(aB - 64, bB - 64), fa[0], fb[0], fa[1], fb[1], y);
        const bool last = rho == 6, more = c + 1 < nch;
        aB += last ? (more ? KT_SZ + 384 : 384) : -128; bB += last ? (more ? X_SZ + 384 : 384) : -128;
        conv_lo1(caddr(aB, bB), fa[1], fb[1], fa[0], fb[0], y);
      }
      store_y<LO>(rs, y, WS_YT + (c0 + c) * 4096, l15, g);
    }
    lds_post(sm, SY_CONV, lane);
    const bool early = wall(fc == tag);
    CFENCE();
    if (early) { LOAD_AF(KS, AFS); VMW(AFS); } else DRAIN();
    if (nch) SET_FLAG(WS_FD + (c0 + nch - 1) * 64);
    STAMP(6);
    if (!early) { poll_flags(p.ws, fcoff, tag); LOAD_AF(KS, AFS); }
    lds_wait(sm, SY_G1DONE, 4);
    gemm2<true, KS>(rs, sm, wave, k0, lane, rt2, nch, bw, af, res);
  } else {
    const int s = wave - 4;
    const bool hasch = s < nch;
    const int ch = hasch ? c0 + s : c0;
    LAS char* KT = sm + LD_KT + s * KT_SZ;
    u32 prm[6][3];
    const u32 po = ch * 384 + lane * 2;
    u32 ldt_raw = gld2(p.ldt, ch * 2), dsk_raw = gld2(p.dsk, ch * 2);
#pragma unroll
    for (int it = 0; it < 3; ++it) {
      prm[0][it] = gld2(p.Are, po + it * 128); prm[1][it] = gld2(p.Aim, po + it * 128); prm[2][it] = gld2(p.Bre, po + it * 128);
      prm[3][it] = gld2(p.Bim, po + it * 128); prm[4][it] = gld2(p.Cre, po + it * 128); prm[5][it] = gld2(p.Cim, po + it * 128);
    }
    asm volatile("s_waitcnt vmcnt(0)" : "+v"(ldt_raw), "+v"(dsk_raw) :: "memory");
    asm volatile("" :: "v"(cpf));
#pragma unroll
    for (int k = 0; k < 6; ++k) asm volatile("" : "+v"(prm[k][0]), "+v"(prm[k][1]), "+v"(prm[k][2]));
    SCHED();
    STAMP(1);
    if (hasch) {
      const int m = l31;
      const float dt = rbf(__builtin_amdgcn_exp2f(bflo(ldt_raw) * 1.44269504089f));
      cf cc[3]; float mx = 0.f;
#pragma unroll
      for (int it = 0; it < 3; ++it) {
        const cf Bc = {bflo(prm[2][it]), bflo(prm[3][it])}, Cc = {bflo(prm[4][it]), bflo(prm[5][it])};
        cc[it] = cmul(Cc, Bc);
        mx = __builtin_fmaxf(mx, __builtin_fmaxf(__builtin_fabsf(cc[it].x), __builtin_fabsf(cc[it].y)));
      }
      const int ex = __builtin_amdgcn_frexp_expf(max64(mx));
      const float ksc = __builtin_amdgcn_ldexpf(dt, ex - 10);
      f32x16 kacc;
#pragma unroll
      for (int i = 0; i < 16; ++i) kacc[i] = 0.f;
      const int baseA = 512 * h + 16 * ((m >> 2) ^ (h << 1)) + 4 * (m & 3);
      const int baseB = 4096 + 2048 * h + 16 * (m >> 2) + 4 * (m & 3);
      const int rdA = m * 128, rsw = (m >> 1) & 7;
      u32x4 ta[4], tb[4];
#define KMFMA(ks) kacc = MFMA32H(__builtin_bit_cast(f16x8, ta[ks]), __builtin_bit_cast(f16x8, tb[ks]), kacc)
#define WRA(rr, v) (*(LAS u32*)(KT + ((baseA ^ (16 * ((((rr) & 3) >> 1) | ((((rr) >> 2) & 1) << 2)))) + 128 * (((rr) & 3) + 8 * ((rr) >> 2)))) = (v))
#define WRB(be, v) (*(LAS u32*)(KT + ((baseB ^ (16 * ((be) >> 1))) + 128 * (be))) = (v))
#define SWAP(dst, a0, a1) do { auto q_ = __builtin_amdgcn_permlane32_swap((a0), (a1), false, false); dst[0] = q_[0]; dst[1] = q_[1]; } while (0)
#pragma unroll
      for (int it = 0; it < 3; ++it) {
        const float lr = dt * bflo(prm[0][it]) * 1.44269504089f, ph = dt * bflo(prm[1][it]) * 0.15915494309f;
        const cf c = {__builtin_amdgcn_ldexpf(cc[it].x, 10 - ex), __builtin_amdgcn_ldexpf(cc[it].y, 10 - ex)};
        const cf e1 = cexp2r(lr, ph), e128 = cexp2r(lr * 128.f, ph * 128.f);
        const cf e2 = csq(e1), e256 = csq(e128), e4 = csq(e2), e512 = csq(e256);
        SCHED(); if (it) KMFMA(0); SCHED();
        const cf e8 = csq(e4), e1024 = csq(e512), e16 = csq(e8), e32 = csq(e16);
        SCHED(); if (it) KMFMA(1); SCHED();
        const cf e64 = csq(e32), c512 = cmul(c, e512), c1024 = cmul(c, e1024);
        const u32 g1 = toh(e1), g2 = toh(e2), g4 = toh(e4), g8 = toh(e8), g16 = toh(e16);
        SCHED(); if (it) KMFMA(2); SCHED();
        const u32 g256 = toh(e256), g512 = toh(e512), k32 = tohc(e32), k64 = tohc(e64), k128 = tohc(e128);
        const u32 g3 = hmul(g2, g1), g24 = hmul(g16, g8), k96 = hmul(k64, k32);
        const u32 b0 = toh(c), b7 = toh(c512), b10 = toh(c1024);
        SCHED(); if (it) KMFMA(3); SCHED();
        u32 R0[2], R7[2], R10[2], P[4][2], H1[2], H2[2], H3[2], H4[2], H8[2], H16[2], H24[2], H256[2];
        SWAP(R0, b0, hmul(b0, g4)); SWAP(R7, b7, hmul(b7, g256)); SWAP(R10, b10, hmul(b10, g512));
        SWAP(P[0], 0x00003C00u, k128); SWAP(P[1], k32, hmul(k128, k32)); SWAP(P[2], k64, hmul(k128, k64)); SWAP(P[3], k96, hmul(k128, k96));
        SWAP(H1, g1, g1); SWAP(H2, g2, g2); SWAP(H3, g3, g3); SWAP(H4, g4, g4); SWAP(H8, g8, g8); SWAP(H16, g16, g16); SWAP(H24, g24, g24); SWAP(H256, g256, g256);
        SCHED();
#pragma unroll
        for (int sp = 0; sp < 2; ++sp) {
#pragma unroll
          for (int a = 0; a < 4; ++a) {
            WRB(4 * a, P[a][sp]); WRB(4 * a + 1, hmulc(P[a][sp], H8[sp])); SCHED();
            WRB(4 * a + 2, hmulc(P[a][sp], H16[sp])); WRB(4 * a + 3, hmulc(P[a][sp], H24[sp])); SCHED();
            if (sp) { KMFMA(a); SCHED(); }
          }
          const u32 r4 = hmul(R0[sp], H256[sp]), r12 = hmul(R10[sp], H256[sp]);
          WRA(0, R0[sp]); WRA(1, hmul(R0[sp], H1[sp])); SCHED();
          WRA(2, hmul(R0[sp], H2[sp])); WRA(3, hmul(R0[sp], H3[sp])); SCHED();
          WRA(4, r4); WRA(5, hmul(r4, H2[sp])); SCHED();
          WRA(6, hmul(r4, H4[sp])); WRA(7, R7[sp]); SCHED();
          WRA(8, hmul(R7[sp], H4[sp])); WRA(9, hmul(R7[sp], H8[sp])); SCHED();
          WRA(10, R10[sp]); WRA(11, hmul(R10[sp], H8[sp])); SCHED();
          WRA(12, r12); WRA(13, hmul(r12, H8[sp]));
          CFENCE(); SCHED();
#pragma unroll
          for (int ks = 0; ks < 4; ++ks) {
            const int so = rdA + 16 * ((2 * ks + h) ^ rsw);
            ta[ks] = *(LAS u32x4*)(KT + so); tb[ks] = *(LAS u32x4*)(KT + 4096 + so);
          }
          CFENCE(); SCHED();
        }
      }
      KMFMA(0); KMFMA(1); KMFMA(2); KMFMA(3);
      CFENCE();
      STAMP(2);
      {
        const int bb = m;
        float kv[14];
#pragma unroll
        for (int i = 0; i < 14; ++i) kv[i] = kacc[i] * ksc;
        u32x2 s0 = {pk2(kv[3], kv[2]), pk2(kv[1], kv[0])};
        *(LAS u32x2*)(KT + 2 * (2044 - 8 * bb - 4 * h)) = s0;
        u32x2 s1 = {pk2(kv[6], kv[5]), pk2(kv[4], kv[4])};
        *(LAS u32x2*)(KT + 2 * (2044 - 256 - 8 * bb - 4 * h)) = s1;
        u32x4 s2 = {pk2(kv[9], kv[9]), pk2(kv[8], kv[8]), pk2(kv[8], kv[7]), pk2(kv[7], kv[7])};
        *(LAS u32x4*)(KT + 2 * (2040 - 512 - 256 * h - 8 * bb)) = s2;
#pragma unroll
        for (int a2 = 0; a2 < 2; ++a2) {
          const float v0 = kv[10 + 2 * a2], v8 = kv[11 + 2 * a2], tie = (bb & 1) ? v8 : v0;
          u32x4 s3 = {pk2(v8, v8), pk2(v8, tie), pk2(v0, v0), pk2(v0, v0)};
          *(LAS u32x4*)(KT + 2 * (2040 - 1024 - 512 * h - 256 * a2 - 8 * bb)) = s3;
        }
        u32x4 z = {0, 0, 0, 0};
        if (lane < 4) *(LAS u32x4*)(KT + 4096 + (lane & 1) * 16 + (lane >> 1) * KC1) = z;
        CFENCE();
        u32 dw[17];
#pragma unroll
        for (int k = 0; k < 4; ++k) { u32x4 v = *(LAS u32x4*)(KT + lane * 64 + k * 16); dw[4 * k] = v.x; dw[4 * k + 1] = v.y; dw[4 * k + 2] = v.z; dw[4 * k + 3] = v.w; }
        dw[16] = *(LAS u32*)(KT + lane * 64 + 64);
#pragma unroll
        for (int k = 0; k < 4; ++k) {
          u32x4 v;
#pragma unroll
          for (int e = 0; e < 4; ++e) v[e] = __builtin_amdgcn_alignbit(dw[4 * k + e + 1], dw[4 * k + e], 16);
          *(LAS u32x4*)(KT + KC1 + lane * 64 + k * 16) = v;
        }
      }
      lds_post(sm, SY_KRDY, lane);
    }
    STAMP(3);
    tag = get_tag(sm);
    u32x4 bw[KS * 5], af[KS * 4];
    lds_wait(sm, SY_GISS, 1);
    GATHER();
    poll_flags(p.ws, fwoff, tag);
    if (hasch) {
      f32x4 y[8 - LO];
      const int par = (l15 & 1) ^ 1;
      u32 aB = (u32)(size_t)KT + (par ? KC1 : 0) + 2 * (2031 - l15 + 8 * g - par) - 3584;
      u32 bB = smb + LD_X + s * X_SZ + 16 * (28 + 2 * l15 + g);
      u32x4 fa[8], fb[8];
      lds_wait(sm, SY_XRDY, 4);
      { const CAddr c = caddr(aB, bB);
#pragma unroll
        for (int q = 0; q < 8; ++q) { fb[7 - q] = FB_(c, 7 - q); fa[q] = FA_(c, q); } }
      const u32x4 dg = diag_frag(dsk_raw, l15, g);
      SCHED();
      lds_wait(sm, SY_MISH0 + s, 1);
      PRIO(1);
      STAMP(4);
#pragma unroll
      for (int i = 0; i < 8 - LO; ++i) { f32x4 z = {0, 0, 0, 0}; y[i] = MFMA16(asb(dg), asb(fb[LO + i]), z); }
      conv_hi2(caddr(aB - 64, bB - 64), caddr(aB - 128, bB - 128), fa, fb, y, [&](int i) NOPK { if (SB1) LOAD_BW1(i); });
      conv_hi2(caddr(aB - 192, bB - 192), caddr(aB - 256, bB - 256), fa, fb, y, [&](int i) NOPK { LOAD_BW1(10 + i); if (!SB1) LOAD_BW1(i); });
      u64 fc = ld_flag(p.ws, fcoff);
      conv_hi2(caddr(aB - 320, bB - 320), caddr(aB - 384, bB - 384), fa, fb, y, [&](int i) NOPK {});
      const bool early = wall(fc == tag);
      CFENCE();
      if (early) LOAD_AF(KS, AFS); else fc = ld_flag(p.ws, fcoff);
      conv_hi2(caddr(aB - 448, bB - 448), caddr(aB - 448, bB - 448), fa, fb, y, [&](int i) NOPK {});
      STAMP(5);
      store_y<8 - LO>(rs, y, WS_YT + ch * 4096 + LO * 512, l15, g);
      lds_post(sm, SY_CONV, lane);
      DRAIN();
      SET_FLAG(WS_FD + (640 + ch) * 64);
      STAMP(6);
      if (!early) {
        if (!wall(fc == tag)) poll_flags(p.ws, fcoff, tag);
        CFENCE();
        LOAD_AF(KS, AFS);
      }
    } else {
      lds_wait(sm, SY_MISH0 + s, 1);
      PRIO(1);
      LOAD_BW();
      poll_flags(p.ws, fcoff, tag);
      LOAD_AF(KS, AFS);
    }
    lds_wait(sm, SY_G1DONE, 4);
    gemm2<true, KS>(rs, sm, wave, k0, lane, rt2, nch, bw, af, res);
  }
  if (wave >= 4) {
    STAMP(7);
    const u32 fdoff = WS_FD + ((rt2 >= 4 * LO ? 640 : 0) + ct2 * 80 + own * 20 + (lane < 20 ? lane : 0)) * 64;
    const u64 fd = ld_flag(p.ws, fdoff);
    int orow[3], och[3];
    u32x4 xres[3];
#pragma unroll
    for (int i = 0; i < 3; ++i) {
      const int id = (i < 2 || lane < 32) ? lane + 64 * i : lane;
      orow[i] = (id * 205) >> 11; och[i] = id - orow[i] * 10;
      xres[i] = *(const u32x4*)(p.x + (rt2 * 64 + own * 16 + orow[i]) * 640 + ct2 * 80 + och[i] * 8);
    }
    lds_wait(sm, SY_GPART, 4);
#pragma unroll
    for (int o = 1; o < 4; ++o)
#pragma unroll
      for (int n = 0; n < 5; ++n) res[n] += *(LAS f32x4*)(sm + LD_SP + ((own - o) & 3) * 15360 + ((o - 1) * 5 + n) * 1024 + l16);
    if (!wall(fd == tag)) poll_n(p.ws, fdoff, lane < 20, tag);
    CFENCE();
    STAMP(12);
    const u32 yo = (lane >> 3) * 4096 + (lane & 7) * 16;
    u32x4 yv[3];
#pragma unroll
    for (int i = 0; i < 3; ++i) yv[i] = BLD(yo + ((i == 2 && lane >= 32) ? OOB : 0u), WS_YT + (ct2 * 80 + own * 20 + 8 * i) * 4096 + rt2 * 128);
#pragma unroll
    for (int st = 0; st < 2; ++st) {
      const int v = (own - 1 - st) & 3;
      lds_wait(sm, SY_SP0 + v, st + 1);
#pragma unroll
      for (int n = 0; n < 5; ++n) res[n] += *(LAS f32x4*)(sm + v * 15360 + (st * 5 + n) * 1024 + l16);
    }
#pragma unroll
    for (int i = 0; i < 3; ++i) if (i < 2 || lane < 32) *(LAS u32x4*)(sm + LD_X + own * 2560 + i * 1024 + l16) = yv[i];
    lds_post(sm, SY_YST, lane);
    lds_wait(sm, SY_SP0 + ((own + 1) & 3), 3);
#pragma unroll
    for (int n = 0; n < 5; ++n) res[n] += *(LAS f32x4*)(sm + ((own + 1) & 3) * 15360 + (10 + n) * 1024 + l16);
    lds_wait(sm, SY_YST, 4);
#pragma unroll
    for (int n = 0; n < 5; ++n) {
      bf16x4 yy = trd(sm + LD_X + (n * 16 + 4 * g + (l15 >> 2)) * 128 + ((lane & 3) >> 1) * 64 + own * 16 + (lane & 1) * 8);
      u32x2 yu = __builtin_bit_cast(u32x2, yy);
      res[n] += f32x4{bflo(yu.x), bfhi(yu.x), bflo(yu.y), bfhi(yu.y)};
    }
    const __amdgpu_buffer_rsrc_t ro = __builtin_amdgcn_make_buffer_rsrc(p.out, 0, L_ * D_ * 2, 0x00020000);
    LAS char* stg = sm + LD_SP + own * 15360;
    STAMP(8);
    lds_wait(sm, SY_SP0 + own, 4);
    STAMP(9);
#pragma unroll
    for (int n = 0; n < 5; ++n) *(LAS f32x4*)(stg + l15 * 336 + n * 64 + g * 16) = res[n] + *(LAS f32x4*)(sm + LD_KT + own * 5120 + n * 1024 + l16);
    CFENCE();
#pragma unroll
    for (int i = 0; i < 3; ++i) {
      f32x4 u = *(LAS f32x4*)(stg + orow[i] * 336 + och[i] * 32), v = *(LAS f32x4*)(stg + orow[i] * 336 + och[i] * 32 + 16);
      u32x4 o = {pk2(u.x + bflo(xres[i].x), u.y + bfhi(xres[i].x)), pk2(u.z + bflo(xres[i].y), u.w + bfhi(xres[i].y)),
                 pk2(v.x + bflo(xres[i].z), v.y + bfhi(xres[i].z)), pk2(v.z + bflo(xres[i].w), v.w + bfhi(xres[i].w))};
      __builtin_amdgcn_raw_buffer_store_b128(o, ro, ((rt2 * 64 + own * 16 + orow[i]) * 640 + ct2 * 80 + och[i] * 8) * 2 + ((i < 2 || lane < 32) ? 0u : OOB), 0, 16);
    }
  }
  STAMP(10);
}

extern "C" void kernel_launch(void* const* d_in, const int* in_sizes, int n_in, void* d_out, int out_size,
                              void* d_ws, size_t ws_size, hipStream_t stream) {
  Params p;
  p.x = (const u16*)d_in[0]; p.Are = (const u16*)d_in[1]; p.Aim = (const u16*)d_in[2]; p.Bre = (const u16*)d_in[3];
  p.Bim = (const u16*)d_in[4]; p.Cre = (const u16*)d_in[5]; p.Cim = (const u16*)d_in[6]; p.ldt = (const u16*)d_in[7];
  p.dsk = (const u16*)d_in[8]; p.w1 = (const u16*)d_in[10]; p.b1 = (const u16*)d_in[11]; p.w2 = (const u16*)d_in[12];
  p.out = (u16*)d_out; p.ws = (char*)d_ws;
  (void)hipFuncSetAttribute((const void*)fused, hipFuncAttributeMaxDynamicSharedMemorySize, LDS_BYTES);
  fused<<<256, 512, LDS_BYTES, stream>>>(p);
}
```

```cpp
#define EV 0
#define EW 0
#include <hip/hip_runtime.h>
#include <stdint.h>
typedef unsigned short u16; typedef unsigned u32; typedef unsigned long long u64;
typedef __attribute__((ext_vector_type(2))) float f32x2;
typedef __attribute__((ext_vector_type(4))) float f32x4;
typedef __attribute__((ext_vector_type(16))) float f32x16;
typedef __attribute__((ext_vector_type(2))) __bf16 bf16x2;
typedef __attribute__((ext_vector_type(4))) __bf16 bf16x4;
typedef __attribute__((ext_vector_type(8))) __bf16 bf16x8;
typedef __attribute__((ext_vector_type(2))) _Float16 f16x2;
typedef __attribute__((ext_vector_type(8))) _Float16 f16x8;
typedef __attribute__((ext_vector_type(2))) u32 u32x2;
typedef __attribute__((ext_vector_type(3))) u32 u32x3;
typedef __attribute__((ext_vector_type(4))) u32 u32x4;
#define LAS __attribute__((address_space(3)))
#define GAS __attribute__((address_space(1)))
#define RLX_AGENT __ATOMIC_RELAXED, __HIP_MEMORY_SCOPE_AGENT
#define RLX_WG __ATOMIC_RELAXED, __HIP_MEMORY_SCOPE_WORKGROUP
#if defined(__HIP_DEVICE_COMPILE__)
#define NOPK __attribute__((target("no-packed-fp32-ops")))
#else
#define NOPK
#endif
#define DEV __device__ __forceinline__ NOPK
#define CFENCE() asm volatile("" ::: "memory")
#define DRAIN() asm volatile("s_waitcnt vmcnt(0)" ::: "memory")
#define LDRAIN() asm volatile("s_waitcnt lgkmcnt(0)" ::: "memory")
#define VMW(n) asm volatile("s_waitcnt vmcnt(%0)" :: "i"(n) : "memory")
#define SCHED() __builtin_amdgcn_sched_barrier(0)
#define PRIO(n) __builtin_amdgcn_s_setprio(n)
#define DMA_G 14
#define BWS 1
#define G2MAP 0
#define GATE 0
#define GATEW 0
#define PW7 2
#define DTH 24
#define DTH1 12
#define DTH2 12
#define AW 6
#define AR 2
#define AFW 6
#define BTH 12
#define BWG 0
#define ROT1 1
#define ROT2 1
#define G1MAP 1
#define FTG 0
#define LSLEEP 2
#define SGATE SY_FTG
#define DSW 12
#define CODEPF 39000
#define XGATE 2
#define DB 2
#define YK 5
#define GTH 6
#define BGT 16
#define KG 8
#define KS (12 - KG)
#define AFG 10
#define AFS 8
#define BT0 18
#define BMI 18
#define SB1 0
#define MG 3
#define PSLEEP 4

constexpr int L_ = 2048, D_ = 640, N_ = 192, F_ = 1536;
constexpr u32 WS_EPOCH = 0;
constexpr u32 WS_FT = 4096;
constexpr u32 WS_FW = WS_FT + 256 * 64;
constexpr u32 WS_FD = WS_FW + 512 * 64;
constexpr u32 WS_FC = WS_FD + 1280 * 64;
constexpr u32 WS_FA = WS_FC + 1024 * 64;
constexpr u32 WS_XNA = WS_FA + 512 * 64;
constexpr u32 WS_W2F = WS_XNA + L_ * D_ * 2;
constexpr u32 WS_ACT = WS_W2F + F_ * D_ * 2;
constexpr u32 WS_YT = WS_ACT + L_ * F_ * 2;
constexpr u32 WS_END = WS_YT + L_ * D_ * 2;
constexpr u32 WS_STAMP = WS_END;
constexpr u32 OOB = 0x40000000u;
constexpr int KT_SZ = 8320, KC1 = 4160, X_SZ = 4608;
constexpr int LD_KT = 122880, LD_X = LD_KT + 3 * KT_SZ;
constexpr int LD_SYNC = LD_X + 3 * X_SZ;
constexpr int LDS_BYTES = 163840;
constexpr int LD_SP = 61440;
constexpr int LNS = 1296;
enum { SY_W1 = 0, SY_G1DONE, SY_FTG, SY_TAGLO, SY_TAGHI, SY_TAGOK, SY_KRDY, SY_CONV, SY_MISH0, SY_MISH1, SY_MISH2, SY_MISH3,
       SY_XRDY, SY_GISS, SY_XT, SY_AISS, SY_GPART, SY_YST, SY_SP0, SY_SP1, SY_SP2, SY_SP3, SY_FAOK, SY_RT, SY_FA0, SY_FA1, SY_GRD };
constexpr u32 SPIN_CAP = 1u << 18;

struct Params {
  const u16 *x, *Are, *Aim, *Bre, *Bim, *Cre, *Cim, *ldt, *dsk, *w1, *b1, *w2;
  u16* out; char* ws;
};

DEV float asf(u32 v) { return __builtin_bit_cast(float, v); }
DEV u32 asu(float v) { return __builtin_bit_cast(u32, v); }
DEV float bflo(u32 v) { return asf(v << 16); }
DEV float bfhi(u32 v) { return asf(v & 0xffff0000u); }
DEV u32 pk2(float a, float b) { f32x2 v = {a, b}; return __builtin_bit_cast(u32, __builtin_convertvector(v, bf16x2)); }
DEV float rbf(float a) { return bflo(pk2(a, a)); }
struct cf { float x, y; };
DEV cf cmul(cf a, cf b) { cf r = {__builtin_fmaf(-a.y, b.y, a.x * b.x), __builtin_fmaf(a.y, b.x, a.x * b.y)}; return r; }
DEV cf cmulc(cf a, cf b) { cf r = {__builtin_fmaf(a.y, b.y, a.x * b.x), __builtin_fmaf(a.y, b.x, -(a.x * b.y))}; return r; }
DEV cf csq(cf a) { cf r = {__builtin_fmaf(-a.y, a.y, a.x * a.x), (a.x + a.x) * a.y}; return r; }
DEV cf cexp2r(float lr, float ph) {
  float e = __builtin_amdgcn_exp2f(lr); float f = __builtin_amdgcn_fractf(ph);
  cf r = {e * __builtin_amdgcn_cosf(f), e * __builtin_amdgcn_sinf(f)}; return r; }
DEV cf csel(bool h, cf a) { cf r = {h ? a.x : 1.f, h ? a.y : 0.f}; return r; }
DEV u32 toh(cf a) { f32x2 v = {a.x, a.y}; return __builtin_bit_cast(u32, __builtin_convertvector(v, f16x2)); }
DEV u32 tohc(cf a) { f32x2 v = {a.x, -a.y}; return __builtin_bit_cast(u32, __builtin_convertvector(v, f16x2)); }
DEV u32 hmul(u32 a_, u32 b_) {
  const f16x2 a = __builtin_bit_cast(f16x2, a_), b = __builtin_bit_cast(f16x2, b_), nb = -b;
  const f16x2 s = __builtin_shufflevector(nb, b, 1, 2);
  return __builtin_bit_cast(u32, __builtin_elementwise_fma(a.yy, s, a.xx * b)); }
DEV u32 hmulc(u32 a_, u32 b_) {
  const f16x2 a = __builtin_bit_cast(f16x2, a_), b = __builtin_bit_cast(f16x2, b_), nb = -b;
  const f16x2 s = __builtin_shufflevector(b, nb, 0, 3);
  return __builtin_bit_cast(u32, __builtin_elementwise_fma(a.yy, b.yx, a.xx * s)); }
template <int CTRL> DEV float dppf(float v) { return asf(__builtin_amdgcn_update_dpp(0, asu(v), CTRL, 0xf, 0xf, true)); }
DEV float red8(float v) { v += dppf<0xB1>(v); v += dppf<0x4E>(v); v += dppf<0x141>(v); return v; }
DEV float red_mod4(float v) {
  v += dppf<0x124>(v); v += dppf<0x128>(v);
  auto r = __builtin_amdgcn_permlane16_swap(asu(v), asu(v), false, false);
  v = asf(r[0]) + asf(r[1]);
  auto q = __builtin_amdgcn_permlane32_swap(asu(v), asu(v), false, false);
  return asf(q[0]) + asf(q[1]); }
DEV float max64(float v) {
  v = __builtin_fmaxf(v, dppf<0xB1>(v)); v = __builtin_fmaxf(v, dppf<0x4E>(v)); v = __builtin_fmaxf(v, dppf<0x141>(v)); v = __builtin_fmaxf(v, dppf<0x140>(v));
  auto r = __builtin_amdgcn_permlane16_swap(asu(v), asu(v), false, false);
  v = __builtin_fmaxf(asf(r[0]), asf(r[1]));
  auto q = __builtin_amdgcn_permlane32_swap(asu(v), asu(v), false, false);
  return __builtin_fmaxf(asf(q[0]), asf(q[1])); }
DEV bool wall(bool c) { return __builtin_amdgcn_ballot_w64(c) == ~0ull; }

DEV u32 lds_ld(LAS char* s, int w) { return __hip_atomic_load((LAS u32*)(s + LD_SYNC) + w, RLX_WG); }
DEV void lds_st(LAS char* s, int w, u32 v) { __hip_atomic_store((LAS u32*)(s + LD_SYNC) + w, v, RLX_WG); }
DEV void lds_wait(LAS char* s, int w, u32 want) {
#pragma unroll 1
  for (u32 n = 0; n < (1u << 22); ++n) { if (__builtin_amdgcn_readfirstlane(lds_ld(s, w)) >= want) break; __builtin_amdgcn_s_sleep(LSLEEP); }
  CFENCE();
}
DEV void lds_post(LAS char* s, int w, int lane) {
  LDRAIN();
  if (lane == 0) (void)__hip_atomic_fetch_add((LAS u32*)(s + LD_SYNC) + w, 1u, RLX_WG);
  CFENCE();
}
DEV u32 lds_arrive(LAS char* s, int w, int lane) {
  LDRAIN();
  u32 r = 0; if (lane == 0) r = __hip_atomic_fetch_add((LAS u32*)(s + LD_SYNC) + w, 1u, RLX_WG);
  CFENCE();
  return __builtin_amdgcn_readfirstlane(r);
}
DEV void lds_post_nw(LAS char* s, int w, int lane) {
  CFENCE();
  if (lane == 0) (void)__hip_atomic_fetch_add((LAS u32*)(s + LD_SYNC) + w, 1u, RLX_WG);
  CFENCE();
}
DEV u64 get_tag(LAS char* s) {
  lds_wait(s, SY_TAGOK, 1);
  return (u64)__builtin_amdgcn_readfirstlane(lds_ld(s, 32)) | ((u64)__builtin_amdgcn_readfirstlane(lds_ld(s, 33)) << 32);
}
DEV u64 ld_flag(char* ws, u32 off) { return __hip_atomic_load((GAS u64*)(ws + off), RLX_AGENT); }
DEV void poll_flags(char* ws, u32 off, u64 tag) {
#pragma unroll 1
  for (u32 n = 0; n < SPIN_CAP; ++n) {
    if (wall(ld_flag(ws, off) == tag)) break;
    __builtin_amdgcn_s_sleep(PSLEEP);
  }
  CFENCE();
}
DEV void poll_n(char* ws, u32 off, bool act, u64 tag) {
#pragma unroll 1
  for (u32 n = 0; n < SPIN_CAP; ++n) {
    u64 v = tag; if (act) v = ld_flag(ws, off);
    if (wall(v == tag)) break;
    __builtin_amdgcn_s_sleep(PSLEEP);
  }
  CFENCE();
}
template <int NL> DEV void polln(char* ws, u32 base, int lane, int nvalid, u64 tag) {
#pragma unroll 1
  for (u32 n = 0; n < SPIN_CAP; ++n) {
    bool ok = true;
#pragma unroll
    for (int k = 0; k < NL; ++k) { int i = lane + 64 * k; u64 v = ld_flag(ws, base + i * 64); ok &= (v == tag) | (i >= nvalid); }
    if (wall(ok)) break;
    __builtin_amdgcn_s_sleep(32);
  }
  CFENCE();
}
DEV bf16x4 trd(LAS char* p) { return __builtin_amdgcn_ds_read_tr16_b64_v4bf16((LAS bf16x4*)p); }
DEV bf16x8 cat(bf16x4 a, bf16x4 b) { return __builtin_shufflevector(a, b, 0, 1, 2, 3, 4, 5, 6, 7); }
DEV bf16x8 asb(u32x4 v) { return __builtin_bit_cast(bf16x8, v); }
DEV LAS char* lp(u32 a) { return (LAS char*)(size_t)a; }
#define MFMA16(a, b, c) __builtin_amdgcn_mfma_f32_16x16x32_bf16(a, b, c, 0, 0, 0)
#define MFMA32(a, b, c) __builtin_amdgcn_mfma_f32_32x32x16_bf16(a, b, c, 0, 0, 0)
#define MFMA32H(a, b, c) __builtin_amdgcn_mfma_f32_32x32x16_f16(a, b, c, 0, 0, 0)
#define BLD(voff, soff) __builtin_amdgcn_raw_buffer_load_b128(rs, (voff), (soff), 16)
#define BST(v, voff, soff) __builtin_amdgcn_raw_buffer_store_b128((v), rs, (voff) + (soff), 0, 16)

#define SET_FLAG(off) do { const u32x4 f_ = {(u32)tag, (u32)(tag >> 32), (u32)tag, (u32)(tag >> 32)}; BST(f_, l16 + (lane < 4 ? 0u : OOB), (off)); } while (0)

#define STAMP(i) do { if ((i) == EV) t_ev = __builtin_amdgcn_s_memrealtime(); } while (0)

#define STAMPX(i) STAMP(i)
DEV void setm0(u32 v) { asm volatile("s_mov_b32 m0, %0\n\ts_nop 0" :: "s"(v) : "memory"); }
template <int OFF> DEV void glds(const void* sbase, u32 voff) {
  asm volatile("global_load_lds_dwordx4 %0, %1 offset:%2" :: "v"(voff), "s"(sbase), "i"(OFF) : "memory");
}
DEV u32x4 ldA(u32 a) { LAS u32* ap = (LAS u32*)lp(a); u32x4 v = {ap[0], ap[1], ap[2], ap[3]}; return v; }
DEV u32x4 ldB(u32 a) { return *(LAS u32x4*)lp(a); }
struct CAddr { u32 a[4]; u32 b; };
DEV CAddr caddr(u32 aB, u32 bB) {
  CAddr c;
#pragma unroll
  for (int i = 0; i < 4; ++i) { c.a[i] = aB + 1024 * (3 - i); asm("" : "+v"(c.a[i])); }
  c.b = bB; asm("" : "+v"(c.b));
  return c;
}
#define FA_(c, q) ldA((c).a[(q) >> 1] + (((q) & 1) ? 0 : 512))
#define FB_(c, q) ldB((c).b + 512 * (q))
constexpr int LO = 3;
template <class H> DEV void conv_hi2(const CAddr& n1, const CAddr& n2, u32x4 (&fa)[8], u32x4 (&fb)[8], f32x4 (&y)[8 - LO], H hook) {
#pragma unroll
  for (int q = 0; q < 8; ++q) {
#pragma unroll
    for (int pp = 7 - q; pp >= 0 && pp + q >= LO; --pp) {
      y[q + pp - LO] = MFMA16(asb(fa[q]), asb(fb[pp]), y[q + pp - LO]);
      if (pp == 7 - q) { SCHED(); fb[pp] = FB_(n1, pp); SCHED(); }
    }
    SCHED(); fa[q] = FA_(n1, q); if (q < 5) hook(q); SCHED();
  }
#pragma unroll
  for (int pp = 7; pp >= 0; --pp) {
#pragma unroll
    for (int q = (pp < LO ? LO - pp : 0); q <= 7 - pp; ++q) {
      y[q + pp - LO] = MFMA16(asb(fa[q]), asb(fb[pp]), y[q + pp - LO]);
      if (pp == (q < LO ? LO - q : 0)) { SCHED(); fa[q] = FA_(n2, q); SCHED(); }
    }
    SCHED(); fb[pp] = FB_(n2, pp); if (pp < 5) hook(9 - pp); SCHED();
  }
}
DEV void conv_lo1(const CAddr& n, const u32x4 (&ca)[LO], const u32x4 (&cb)[LO], u32x4 (&na)[LO], u32x4 (&nb)[LO], f32x4 (&y)[LO]) {
  int k = 0;
#pragma unroll
  for (int q = 0; q < LO; ++q)
#pragma unroll
    for (int pp = LO - 1 - q; pp >= 0; --pp) {
      y[q + pp] = MFMA16(asb(ca[q]), asb(cb[pp]), y[q + pp]);
      SCHED();
      if (k < 2 * LO) { if (k & 1) nb[LO - 1 - (k >> 1)] = FB_(n, LO - 1 - (k >> 1)); else na[k >> 1] = FA_(n, k >> 1); }
      ++k;
      SCHED();
    }
}
DEV u32x4 diag_frag(u32 dsk, int l15, int g) {
  const u32 v = (g == 2 + (l15 >> 3)) ? ((l15 & 1) ? dsk << 16 : dsk) : 0u;
  const int w = (l15 & 7) >> 1;
  u32x4 r = {w == 0 ? v : 0u, w == 1 ? v : 0u, w == 2 ? v : 0u, w == 3 ? v : 0u};
  return r;
}
template <int NT> DEV void store_y(const __amdgpu_buffer_rsrc_t rs, const f32x4 (&y)[NT], u32 base, int l15, int g) {
#pragma unroll
  for (int t = 0; t < NT; t += 2) {
    const int u = t + 1 < NT ? t + 1 : t;
    const u32 a0 = pk2(y[t].x, y[t].y), a1 = pk2(y[t].z, y[t].w), b0 = pk2(y[u].x, y[u].y), b1 = pk2(y[u].z, y[u].w);
    auto s0 = __builtin_amdgcn_permlane16_swap(a0, b0, false, false);
    auto s1 = __builtin_amdgcn_permlane16_swap(a1, b1, false, false);
    const u32x4 o = {s0[0], s1[0], s0[1], s1[1]};
    BST(o, t * 512 + ((g & 1) ? (u > t ? 512u : OOB) : 0u) + (l15 >> 2) * 128 + (g >> 1) * 64 + (l15 & 3) * 16, base);
  }
}
DEV u32 gld2(const void* sbase, u32 voff) { u32 r; asm volatile("global_load_ushort %0, %1, %2" : "=&v"(r) : "v"(voff), "s"(sbase) : "memory"); return r; }

#define LOAD_AF1(NK, u) af[u] = BLD(l16 + (((u) % NK) & 3) * 1024, WS_ACT + ((rt2 * 4 + ((own + 1 + (u) / NK) & 3)) * 48 + k0 + (((u) % NK) & ~3)) * 1024)
template <bool SW, int NK>
DEV void gemm2(const __amdgpu_buffer_rsrc_t rs, LAS char* sm, int wave, int k0, int lane, int rt2, int nch,
               u32x4 (&bw)[NK * 5], u32x4 (&af)[NK * 4], f32x4 (&res)[5]) {
  constexpr int W0 = SW ? AFS : AFG, RP = SW ? 2 : 1;
  const int own = wave & 3;
  const u32 l16 = lane * 16;
  LAS char* dst = sm + wave * 15360 + l16;
  f32x4 acc[2][5];
#pragma unroll
  for (int st = 0; st < 4; ++st) {
#pragma unroll
    for (int k = 0; k < NK; ++k) {
#pragma unroll
      for (int n = 0; n < 5; ++n) {
        f32x4 z = {0, 0, 0, 0};
        acc[st & 1][n] = MFMA16(asb(bw[k * 5 + n]), asb(af[st * NK + k]), k ? acc[st & 1][n] : z);
        SCHED();
        if (n < RP && (st * NK + k) * RP + n + W0 < 4 * NK) LOAD_AF1(NK, (st * NK + k) * RP + n + W0);
        if (st > 0 && n >= 2 && (n & 1) == 0 && k * 2 + (n >> 2) < 5) *(LAS f32x4*)(dst + ((st - 1) * 5 + k * 2 + (n >> 2)) * 1024) = acc[(st - 1) & 1][k * 2 + (n >> 2)];
        SCHED();
      }
      if (st > 0 && k == 2) { if (!SW) lds_post_nw(sm, SY_SP0 + own, lane); else if (st == 3) lds_post_nw(sm, SY_GPART, lane); }
      SCHED();
    }
  }
  if (!SW) {
    lds_wait(sm, SY_CONV, nch + 1);
#pragma unroll
    for (int n = 0; n < 5; ++n) *(LAS f32x4*)(sm + LD_KT + own * 5120 + n * 1024 + l16) = acc[1][n];
    lds_post_nw(sm, SY_SP0 + own, lane);
  } else {
#pragma unroll
    for (int n = 0; n < 5; ++n) res[n] = acc[1][n];
  }
}

__global__ __launch_bounds__(512, 2) NOPK void fused(Params p) {
  extern __shared__ __attribute__((aligned(16))) char smem_[];
  LAS char* sm = (LAS char*)smem_;
  const u32 smb = (u32)(size_t)sm;
  const int b = __builtin_amdgcn_workgroup_id_x(), xcd = b & 7, j = b >> 3;
  const int tid = __builtin_amdgcn_workitem_id_x(), wave = __builtin_amdgcn_readfirstlane(tid >> 6), lane = tid & 63;
  const int g = lane >> 4, l15 = lane & 15, l31 = lane & 31, h = lane >> 5;
  const u32 l16 = lane * 16;
  const __amdgpu_buffer_rsrc_t rs = __builtin_amdgcn_make_buffer_rsrc(p.ws, 0, WS_END, 0x00020000);
  u64 t_ev = 0;
  STAMP(0);
  u32 cpf;
  {
    const char* pc = (const char*)(__builtin_amdgcn_s_getpc() & ~255ull);
    u32 o = (j * 8 + wave) * 256 + (lane & 1) * 128;
    if (o > CODEPF - 128) o = 0;
    asm volatile("global_load_dword %0, %1, %2" : "=&v"(cpf) : "v"(o), "s"(pc) : "memory");
  }
  asm volatile("" :: "s"(p.x), "s"(p.ws), "s"(p.w1));
  const int lnb = xcd * 32 + j;
  u32x4 xv[5];
  u64 ep;
  if (wave < 2) {
    asm volatile("global_load_dwordx2 %0, %1, %2 sc1" : "=&v"(ep) : "v"(0u), "s"(p.ws + WS_EPOCH) : "memory");
#pragma unroll
    for (int i = 0; i < 5; ++i)
      asm volatile("global_load_dwordx4 %0, %1, %2 offset:%3" : "=&v"(xv[i]) : "v"((lane & 3) * 1280 + (lane >> 2) * 16), "s"(p.x + (lnb * 8 + wave * 4) * 640), "i"(i * 256) : "memory");
  }
  if (tid < 32) lds_st(sm, tid, 0);
  LDRAIN();
  __builtin_amdgcn_s_barrier();
  SCHED();
  u64 tag;
  if (wave < 2) {
      const int gw = wave;
      PRIO(3);
      asm volatile("s_waitcnt vmcnt(0)" : "+v"(xv[0]), "+v"(xv[1]), "+v"(xv[2]), "+v"(xv[3]), "+v"(xv[4]), "+v"(ep) :: "memory");
      lds_post_nw(sm, SY_FAOK, lane);
      const bf16x2 ones = __builtin_bit_cast(bf16x2, 0x3F803F80u);
      float s2[2] = {0.f, 0.f}, q2[2] = {0.f, 0.f};
#pragma unroll
      for (int i = 0; i < 5; ++i)
#pragma unroll
        for (int k = 0; k < 4; ++k) {
          const u32 xw = xv[i][k]; const bf16x2 v = __builtin_bit_cast(bf16x2, xw);
          s2[k & 1] = __builtin_amdgcn_fdot2_f32_bf16(v, ones, s2[k & 1], false);
          q2[k & 1] = __builtin_amdgcn_fdot2_f32_bf16(v, v, q2[k & 1], false);
        }
      const float mean = red_mod4(s2[0] + s2[1]) * (1.f / 640.f);
      const float var = __builtin_fmaxf(__builtin_fmaf(-mean, mean, red_mod4(q2[0] + q2[1]) * (1.f / 640.f)), 0.f);
      const float sd = rbf(__builtin_amdgcn_sqrtf(rbf(rbf(var) + 1e-5f)));
      const float rinv = __builtin_amdgcn_rcpf(sd), nmr = -rbf(mean) * rinv;
#pragma unroll
      for (int i = 0; i < 5; ++i) {
        u32x4 o;
#pragma unroll
        for (int k = 0; k < 4; ++k) o[k] = pk2(__builtin_fmaf(bflo(xv[i][k]), rinv, nmr), __builtin_fmaf(bfhi(xv[i][k]), rinv, nmr));
        BST(o, l16 + i * 1024, WS_XNA + (lnb * 2 + gw) * 5120);
      }
      STAMP(1);
      {
        const u64 t = ep * 0x9E3779B97F4A7C15ull + 0x632BE59BD9B4E019ull;
        tag = (u64)__builtin_amdgcn_readfirstlane((u32)t) | ((u64)__builtin_amdgcn_readfirstlane((u32)(t >> 32)) << 32);
        if (tid == 0) { const u32x4 tv = {(u32)tag, (u32)(tag >> 32), (u32)ep, (u32)(ep >> 32)}; *(LAS u32x4*)(sm + LD_SYNC + 128) = tv; CFENCE(); lds_st(sm, SY_TAGOK, 1); }
      }
      DRAIN();
      SET_FLAG(WS_FA + (lnb * 2 + gw) * 64);
      if (lds_arrive(sm, SY_XT, lane) == 1) SET_FLAG(WS_FT + lnb * 64);
      STAMP(2);
  }
  const int rp = (xcd & 1) * 8 + (j & 7), cs = (xcd >> 1) * 4 + (j >> 3), ri = (j & 7) + 2 * (j >> 3);
  if (wave == 7) {
    PRIO(3);
    u32 so[3];
#pragma unroll
    for (int r = 0; r < 3; ++r) { const int c = r * 64 + lane, row = c / 12, cc = c - row * 12; so[r] = row * 3072 + cc * 16 + 2048 - 1024 * r; }
    lds_wait(sm, SY_FAOK, XGATE);
#pragma unroll 1
    for (int bl = 0; bl < 8; ++bl) {
      const char* src = (const char*)p.w1 + cs * 192 - 2048 + ((bl + ROT1 * ri) & 7) * (80 * 3072);
#pragma unroll
      for (int q = 0; q < 5; ++q) {
        setm0(smb + bl * 15360 + q * 3072);
        glds<0>(src + q * 49152, so[0]); glds<1024>(src + q * 49152, so[1]); glds<2048>(src + q * 49152, so[2]);
      }
      VMW(15 * DB);
      if (bl >= DB && lane == 0) lds_st(sm, SY_W1, 5 * (bl + 1 - DB));
    }
    STAMP(1);
    if (DB > 2) { VMW(30); if (lane == 0) lds_st(sm, SY_W1, 30); }
    if (DB > 1) { VMW(15); if (lane == 0) lds_st(sm, SY_W1, 35); }
    DRAIN();
    if (lane == 0) lds_st(sm, SY_W1, 40);
  }
  const int rt2 = xcd * 4 + (j & 3), ct2 = j >> 2;
  const int jc = (j * 43) >> 7, jr = j - jc * 3;
  const int c0 = j < 30 ? xcd * 80 + jc * 8 + jr * 3 : 0, nch = j < 30 ? (jr < 2 ? 3 : 2) : 0;
  const int own = wave & 3;
  const int rot = ROT1 * 5 * (ri & 7);
  const int kq = (wave + ROT2 * (j + (j >> 2))) & 3;
  const int k0 = kq * 12 + (wave < 4 ? 0 : KG), nk = wave < 4 ? KG : 12 - KG;
  const int kf = k0 + ((lane >> 1) < nk ? (lane >> 1) : 0);
  const u32 fcoff = WS_FC + (rt2 * 32 + ((kf * 43) >> 7) * 2 + (lane & 1)) * 64;
  const u32 fwoff = WS_FW + (kf * 10 + ((ct2 * 80) >> 6) + (lane & 1)) * 64;
  f32x4 res[5];
#define LOAD_BW1(q) bw[q] = BLD(l16 + (((q) % 5) & 3) * 1024, WS_W2F + ((k0 + (q) / 5) * 40 + ct2 * 5 + (((q) % 5) & 4)) * 1024)
#define LOAD_BW() do { _Pragma("unroll") for (int q = 0; q < KS * 5; ++q) { LOAD_BW1(q); if (q & 1) VMW(BTH); } SCHED(); } while (0)
#define LOAD_AF(NK, W) do { _Pragma("unroll") for (int u = 0; u < W; ++u) LOAD_AF1(NK, u); SCHED(); } while (0)
#define GATHER() do { if (nch) { \
      u32x2 gv[8]; \
      const u32 go = (lane >> 2) * 5120 + (xcd * 10 + jc) * 64 + (lane & 3) * 16 + jr * 4; \
      _Pragma("unroll") for (int it = 0; it < 8; ++it) gv[it] = __builtin_amdgcn_raw_buffer_load_b64(rs, go, WS_XNA + (own * 8 + it) * 16 * 5120, 16); \
      const u32 sh = jr * 16; \
      LAS u16* xd = (LAS u16*)(sm + LD_X + 480 + own * 1024 + lane * 2); \
      _Pragma("unroll") for (int it = 0; it < 8; ++it) { \
        const u64 v = (((u64)gv[it].y << 32) | gv[it].x) >> sh; \
        xd[it * 64] = (u16)v; xd[it * 64 + X_SZ / 2] = (u16)(v >> 16); xd[it * 64 + X_SZ] = (u16)(v >> 32); } } \
      lds_post(sm, SY_XRDY, lane); } while (0)

  if (__builtin_expect(wave < 4, 1)) {
    const int gw = wave;
    u32x4 bw[KG * 5], af[KG * 4];
    u32 braw[3];
#pragma unroll
    for (int n = 0; n < 3; ++n) braw[n] = p.b1[cs * 96 + n * 32 + l31];
    if (gw >= 2) {
      PRIO(3);
      const int pid = (lnb < 240 ? lnb * 2 : 0) + (gw - 2), rks = pid / 10, cb = pid - rks * 10;
      const char* src = (const char*)p.w2 + (32 * rks * 640 + 64 * cb) * 2;
      const u32 wo = (lane >> 3) * 1280 + (lane & 7) * 16;
      lds_wait(sm, SY_FAOK, XGATE);
      setm0(smb + LD_X + (gw - 2) * 4096);
      glds<0>(src, wo); glds<1024>(src + 9216, wo); glds<2048>(src + 18432, wo); glds<3072>(src + 27648, wo);
      DRAIN();
      u32x4 wr[4];
#pragma unroll
      for (int n = 0; n < 4; ++n) {
        LAS char* tp = sm + LD_X + (gw - 2) * 4096 + (16 * (g >> 1) + 4 * (g & 1) + (l15 >> 2)) * 128 + 32 * n + 8 * (lane & 3);
        wr[n] = __builtin_bit_cast(u32x4, cat(trd(tp), trd(tp + 1024)));
      }
      lds_post(sm, SY_RT, lane);
#pragma unroll
      for (int n = 0; n < 4; ++n) BST(wr[n], l16 + n * 1024 + (lnb < 240 ? 0u : OOB), WS_W2F + (rks * 40 + 4 * cb) * 1024);
      STAMP(1);
      tag = get_tag(sm);
      DRAIN();
      if (lnb < 240) SET_FLAG(WS_FW + pid * 64);
      STAMP(2);
    }
    if (gw < 2) {
      poll_n(p.ws, WS_FA + (rp * 32 + gw * 8 + (lane & 8) * 2 + (lane & 7)) * 64, lane < 16, tag);
      lds_post_nw(sm, SY_FA0 + gw, lane);
    } else lds_wait(sm, SY_FA0 + (gw & 1), 1);
    asm volatile("" :: "v"(cpf));
    PRIO(2);
    STAMP(3);
    u32x4 a[40];
    const u32 av = (l31 >> 2) * 5120 + h * 64 + (l31 & 3) * 16;
#define LOAD_A(t) a[t] = BLD(av + ((t) % 5) * 128, WS_XNA + (rp * 32 + gw * 8) * 5120 + (((t) / 5 * 5 + rot >= 40) ? (t) / 5 * 5 + rot - 40 : (t) / 5 * 5 + rot) * 128)
#pragma unroll
    for (int t = 0; t < AW; ++t) LOAD_A(t);
    u64 fw = ld_flag(p.ws, fwoff);
    SCHED();
    lds_wait(sm, SY_W1, 5);
    f32x16 acc[3];
    {
      u32 wb[2], wp = 0;
      wb[0] = smb + 192 * (8 * h + (l15 >> 2)) + 32 * (g & 1) + 8 * (lane & 3); wb[1] = wb[0] + 20 * 3072;
      asm("" : "+v"(wb[0]), "+v"(wb[1]));
      bf16x8 wf[3][3];
#define RDW(t, n) wf[(t) % 3][n] = cat(trd(lp(wb[(t) / 20] + ((t) % 20) * 3072 + (n) * 64)), trd(lp(wb[(t) / 20] + ((t) % 20) * 3072 + (n) * 64 + 768)))
#pragma unroll
      for (int t = 0; t < 2; ++t) { RDW(t, 0); RDW(t, 1); RDW(t, 2); }
      const u32x4 xe = {h ? 0u : 0x3F80u, 0u, 0u, 0u};
#pragma unroll
      for (int n = 0; n < 3; ++n) {
        f32x16 z;
#pragma unroll
        for (int i = 0; i < 16; ++i) z[i] = 0.f;
        const u32x4 we = {braw[n], 0u, 0u, 0u};
        acc[n] = MFMA32(asb(we), asb(xe), z);
      }
      SCHED();
#pragma unroll
      for (int t = 0; t < 40; ++t) {
#pragma unroll
        for (int n = 0; n < 3; ++n) {
          acc[n] = MFMA32(wf[t % 3][n], asb(a[t]), acc[n]);
          SCHED();
          if ((t + 2) % 5 == 0 && n == 0 && t + 2 < 40) {
            const u32 need = t + 7;
            if (__builtin_amdgcn_readfirstlane(wp) < need) lds_wait(sm, SY_W1, need);
            CFENCE();
          }
          if (t + 2 < 40) RDW(t + 2, n);
          if ((t + 3) % 5 == 0 && n == 0) wp = lds_ld(sm, SY_W1);
          if (n < AR && AW + t * AR + n < 40) LOAD_A(AW + t * AR + n);
          if (n == 2 && t == BGT) lds_post_nw(sm, SY_AISS, lane);
          if (n == 1 && t == FTG) lds_post_nw(sm, SY_FTG, lane);
          if (t == BT0 - 1 && n == 2) { if (!wall(fw == tag)) poll_flags(p.ws, fwoff, tag); CFENCE(); }
          if (t >= BT0 && n == 1) LOAD_BW1(t - BT0);
          SCHED();
        }
      }
    }
    lds_post_nw(sm, SY_G1DONE, lane);
    STAMP(4);
    {
      const u32 ao = (l31 >> 4) * 48 * 1024 + (h * 16 + l15) * 16;
#pragma unroll
      for (int n = 0; n < 3; ++n)
#pragma unroll
        for (int c2 = 0; c2 < 2; ++c2) {
          float mv[8];
          if (n * 2 + c2 == MG) lds_post_nw(sm, SY_MISH0 + gw, lane);
#pragma unroll
          for (int e = 0; e < 8; ++e) {
            const float hv = acc[n][8 * c2 + e];
            const float ex = __builtin_amdgcn_exp2f(hv * 1.44269504089f);
            const float rc = __builtin_amdgcn_rcpf(__builtin_fmaf(ex, ex + 2.f, 2.f));
            mv[e] = hv * __builtin_fmaf(rc, -2.f, 1.f);
            if (e & 1) {
              const int q = 40 - BT0 + (n * 2 + c2) * 4 + (e >> 1);
              SCHED(); if (q < KG * 5 && q < 40 - BT0 + BMI) LOAD_BW1(q); SCHED();
            }
          }
          u32x4 o = {pk2(mv[0], mv[1]), pk2(mv[2], mv[3]), pk2(mv[4], mv[5]), pk2(mv[6], mv[7])};
          BST(o, ao + n * 1024 + c2 * 512, WS_ACT + ((rp * 8 + gw * 2) * 48 + cs * 3) * 1024);
        }
    }
    if (MG >= 6) lds_post_nw(sm, SY_MISH0 + gw, lane);
    STAMP(5);
    DRAIN();
    SET_FLAG(WS_FC + ((rp * 2 + (gw >> 1)) * 32 + cs * 2 + (gw & 1)) * 64);
    PRIO(0);
    STAMP(6);
#pragma unroll
    for (int q = 40 - BT0 + BMI; q < KG * 5; ++q) LOAD_BW1(q);
    poll_flags(p.ws, fcoff, tag);
    STAMPX(7);
    LOAD_AF(KG, AFG);
    lds_wait(sm, SY_G1DONE, 4);
    gemm2<false, KG>(rs, sm, wave, k0, lane, rt2, nch, bw, af, res);
    STAMPX(11);
  } else if (wave == 7) {
    const u32 dsk3 = p.dsk[c0 + (lane < nch ? lane : 0)];
    asm volatile("" :: "v"(cpf));
    STAMP(2);
    PRIO(3);
    tag = get_tag(sm);
    lds_wait(sm, SY_FTG, 4);
    STAMP(13);
    polln<4>(p.ws, WS_FT, lane, 256, tag);
    if (b == 0 && lane == 0) __hip_atomic_store((GAS u64*)(p.ws + WS_EPOCH), ((u64)lds_ld(sm, 34) | ((u64)lds_ld(sm, 35) << 32)) + 1, RLX_AGENT);
    STAMP(3);
    lds_wait(sm, SY_RT, 2);
    lds_post_nw(sm, SY_GISS, lane);
    PRIO(0);
    if (lane < 30) {
      const u32x4 z = {0, 0, 0, 0};
#pragma unroll
      for (int c = 0; c < 3; ++c) *(LAS u32x4*)(sm + LD_X + c * X_SZ + l16) = z;
    }
    GATHER();
    u32x4 bw[KS * 5], af[KS * 4];
    STAMP(4);
    PRIO(0);
    poll_flags(p.ws, fwoff, tag);
    lds_wait(sm, SY_AISS, 4);
    LOAD_BW();
    lds_wait(sm, SY_KRDY, nch);
    lds_wait(sm, SY_XRDY, 4);
    const int par = (l15 & 1) ^ 1;
    u32 aB = smb + LD_KT + (par ? KC1 : 0) + 2 * (2031 - l15 + 8 * g - par) - 3584;
    u32 bB = smb + LD_X + 16 * (28 + 2 * l15 + g);
    u32x4 fa[2][LO], fb[2][LO];
    { const CAddr c = caddr(aB, bB);
#pragma unroll
      for (int q = 0; q < LO; ++q) { fa[0][q] = FA_(c, q); fb[0][q] = FB_(c, q); } }
    lds_wait(sm, SY_MISH3, 1);
    PRIO(1);
    STAMP(5);
    u64 fc = 0;
#pragma unroll 1
    for (int c = 0; c < nch; ++c) {
      f32x4 y[LO];
      const u32x4 dg = diag_frag(__builtin_amdgcn_readlane(dsk3, c), l15, g);
#pragma unroll
      for (int i = 0; i < LO; ++i) { f32x4 z = {0, 0, 0, 0}; y[i] = MFMA16(asb(dg), asb(fb[0][i]), z); }
      fc = ld_flag(p.ws, fcoff);
#pragma unroll 1
      for (int rho = 0; rho < 8; rho += 2) {
        if (c && rho == 6) { VMW(0); SET_FLAG(WS_FD + (c0 + c - 1) * 64); }
        conv_lo1(caddr(aB - 64, bB - 64), fa[0], fb[0], fa[1], fb[1], y);
        const bool last = rho == 6, more = c + 1 < nch;
        aB += last ? (more ? KT_SZ + 384 : 384) : -128; bB += last ? (more ? X_SZ + 384 : 384) : -128;
        conv_lo1(caddr(aB, bB), fa[1], fb[1], fa[0], fb[0], y);
      }
      store_y<LO>(rs, y, WS_YT + (c0 + c) * 4096, l15, g);
    }
    lds_post(sm, SY_CONV, lane);
    const bool early = wall(fc == tag);
    CFENCE();
    if (early) { LOAD_AF(KS, AFS); VMW(AFS); } else DRAIN();
    if (nch) SET_FLAG(WS_FD + (c0 + nch - 1) * 64);
    STAMP(6);
    if (!early) { poll_flags(p.ws, fcoff, tag); LOAD_AF(KS, AFS); }
    lds_wait(sm, SY_G1DONE, 4);
    gemm2<true, KS>(rs, sm, wave, k0, lane, rt2, nch, bw, af, res);
  } else {
    const int s = wave - 4;
    const bool hasch = s < nch;
    const int ch = hasch ? c0 + s : c0;
    LAS char* KT = sm + LD_KT + s * KT_SZ;
    u32 prm[6][3];
    const u32 po = ch * 384 + lane * 2;
    u32 ldt_raw = gld2(p.ldt, ch * 2), dsk_raw = gld2(p.dsk, ch * 2);
#pragma unroll
    for (int it = 0; it < 3; ++it) {
      prm[0][it] = gld2(p.Are, po + it * 128); prm[1][it] = gld2(p.Aim, po + it * 128); prm[2][it] = gld2(p.Bre, po + it * 128);
      prm[3][it] = gld2(p.Bim, po + it * 128); prm[4][it] = gld2(p.Cre, po + it * 128); prm[5][it] = gld2(p.Cim, po + it * 128);
    }
    asm volatile("s_waitcnt vmcnt(0)" : "+v"(ldt_raw), "+v"(dsk_raw) :: "memory");
    asm volatile("" :: "v"(cpf));
#pragma unroll
    for (int k = 0; k < 6; ++k) asm volatile("" : "+v"(prm[k][0]), "+v"(prm[k][1]), "+v"(prm[k][2]));
    SCHED();
    STAMP(1);
    if (hasch) {
      const int m = l31;
      const float dt = rbf(__builtin_amdgcn_exp2f(bflo(ldt_raw) * 1.44269504089f));
      cf cc[3]; float mx = 0.f;
#pragma unroll
      for (int it = 0; it < 3; ++it) {
        const cf Bc = {bflo(prm[2][it]), bflo(prm[3][it])}, Cc = {bflo(prm[4][it]), bflo(prm[5][it])};
        cc[it] = cmul(Cc, Bc);
        mx = __builtin_fmaxf(mx, __builtin_fmaxf(__builtin_fabsf(cc[it].x), __builtin_fabsf(cc[it].y)));
      }
      const int ex = __builtin_amdgcn_frexp_expf(max64(mx));
      const float ksc = __builtin_amdgcn_ldexpf(dt, ex - 10);
      f32x16 kacc;
#pragma unroll
      for (int i = 0; i < 16; ++i) kacc[i] = 0.f;
      const int baseA = 512 * h + 16 * ((m >> 2) ^ (h << 1)) + 4 * (m & 3);
      const int baseB = 4096 + 2048 * h + 16 * (m >> 2) + 4 * (m & 3);
      const int rdA = m * 128, rsw = (m >> 1) & 7;
      u32x4 ta[4], tb[4];
#define KMFMA(ks) kacc = MFMA32H(__builtin_bit_cast(f16x8, ta[ks]), __builtin_bit_cast(f16x8, tb[ks]), kacc)
#define WRA(rr, v) (*(LAS u32*)(KT + ((baseA ^ (16 * ((((rr) & 3) >> 1) | ((((rr) >> 2) & 1) << 2)))) + 128 * (((rr) & 3) + 8 * ((rr) >> 2)))) = (v))
#define WRB(be, v) (*(LAS u32*)(KT + ((baseB ^ (16 * ((be) >> 1))) + 128 * (be))) = (v))
#define SWAP(dst, a0, a1) do { auto q_ = __builtin_amdgcn_permlane32_swap((a0), (a1), false, false); dst[0] = q_[0]; dst[1] = q_[1]; } while (0)
#pragma unroll
      for (int it = 0; it < 3; ++it) {
        const float lr = dt * bflo(prm[0][it]) * 1.44269504089f, ph = dt * bflo(prm[1][it]) * 0.15915494309f;
        const cf c = {__builtin_amdgcn_ldexpf(cc[it].x, 10 - ex), __builtin_amdgcn_ldexpf(cc[it].y, 10 - ex)};
        const cf e1 = cexp2r(lr, ph), e128 = cexp2r(lr * 128.f, ph * 128.f);
        const cf e2 = csq(e1), e256 = csq(e128), e4 = csq(e2), e512 = csq(e256);
        SCHED(); if (it) KMFMA(0); SCHED();
        const cf e8 = csq(e4), e1024 = csq(e512), e16 = csq(e8), e32 = csq(e16);
        SCHED(); if (it) KMFMA(1); SCHED();
        const cf e64 = csq(e32), c512 = cmul(c, e512), c1024 = cmul(c, e1024);
        const u32 g1 = toh(e1), g2 = toh(e2), g4 = toh(e4), g8 = toh(e8), g16 = toh(e16);
        SCHED(); if (it) KMFMA(2); SCHED();
        const u32 g256 = toh(e256), g512 = toh(e512), k32 = tohc(e32), k64 = tohc(e64), k128 = tohc(e128);
        const u32 g3 = hmul(g2, g1), g24 = hmul(g16, g8), k96 = hmul(k64, k32);
        const u32 b0 = toh(c), b7 = toh(c512), b10 = toh(c1024);
        SCHED(); if (it) KMFMA(3); SCHED();
        u32 R0[2], R7[2], R10[2], P[4][2], H1[2], H2[2], H3[2], H4[2], H8[2], H16[2], H24[2], H256[2];
        SWAP(R0, b0, hmul(b0, g4)); SWAP(R7, b7, hmul(b7, g256)); SWAP(R10, b10, hmul(b10, g512));
        SWAP(P[0], 0x00003C00u, k128); SWAP(P[1], k32, hmul(k128, k32)); SWAP(P[2], k64, hmul(k128, k64)); SWAP(P[3], k96, hmul(k128, k96));
        SWAP(H1, g1, g1); SWAP(H2, g2, g2); SWAP(H3, g3, g3); SWAP(H4, g4, g4); SWAP(H8, g8, g8); SWAP(H16, g16, g16); SWAP(H24, g24, g24); SWAP(H256, g256, g256);
        SCHED();
#pragma unroll
        for (int sp = 0; sp < 2; ++sp) {
#pragma unroll
          for (int a = 0; a < 4; ++a) {
            WRB(4 * a, P[a][sp]); WRB(4 * a + 1, hmulc(P[a][sp], H8[sp])); SCHED();
            WRB(4 * a + 2, hmulc(P[a][sp], H16[sp])); WRB(4 * a + 3, hmulc(P[a][sp], H24[sp])); SCHED();
            if (sp) { KMFMA(a); SCHED(); }
          }
          const u32 r4 = hmul(R0[sp], H256[sp]), r12 = hmul(R10[sp], H256[sp]);
          WRA(0, R0[sp]); WRA(1, hmul(R0[sp], H1[sp])); SCHED();
          WRA(2, hmul(R0[sp], H2[sp])); WRA(3, hmul(R0[sp], H3[sp])); SCHED();
          WRA(4, r4); WRA(5, hmul(r4, H2[sp])); SCHED();
          WRA(6, hmul(r4, H4[sp])); WRA(7, R7[sp]); SCHED();
          WRA(8, hmul(R7[sp], H4[sp])); WRA(9, hmul(R7[sp], H8[sp])); SCHED();
          WRA(10, R10[sp]); WRA(11, hmul(R10[sp], H8[sp])); SCHED();
          WRA(12, r12); WRA(13, hmul(r12, H8[sp]));
          CFENCE(); SCHED();
#pragma unroll
          for (int ks = 0; ks < 4; ++ks) {
            const int so = rdA + 16 * ((2 * ks + h) ^ rsw);
            ta[ks] = *(LAS u32x4*)(KT + so); tb[ks] = *(LAS u32x4*)(KT + 4096 + so);
          }
          CFENCE(); SCHED();
        }
      }
      KMFMA(0); KMFMA(1); KMFMA(2); KMFMA(3);
      CFENCE();
      STAMP(2);
      {
        const int bb = m;
        float kv[14];
#pragma unroll
        for (int i = 0; i < 14; ++i) kv[i] = kacc[i] * ksc;
        u32x2 s0 = {pk2(kv[3], kv[2]), pk2(kv[1], kv[0])};
        *(LAS u32x2*)(KT + 2 * (2044 - 8 * bb - 4 * h)) = s0;
        u32x2 s1 = {pk2(kv[6], kv[5]), pk2(kv[4], kv[4])};
        *(LAS u32x2*)(KT + 2 * (2044 - 256 - 8 * bb - 4 * h)) = s1;
        u32x4 s2 = {pk2(kv[9], kv[9]), pk2(kv[8], kv[8]), pk2(kv[8], kv[7]), pk2(kv[7], kv[7])};
        *(LAS u32x4*)(KT + 2 * (2040 - 512 - 256 * h - 8 * bb)) = s2;
#pragma unroll
        for (int a2 = 0; a2 < 2; ++a2) {
          const float v0 = kv[10 + 2 * a2], v8 = kv[11 + 2 * a2], tie = (bb & 1) ? v8 : v0;
          u32x4 s3 = {pk2(v8, v8), pk2(v8, tie), pk2(v0, v0), pk2(v0, v0)};
          *(LAS u32x4*)(KT + 2 * (2040 - 1024 - 512 * h - 256 * a2 - 8 * bb)) = s3;
        }
        u32x4 z = {0, 0, 0, 0};
        if (lane < 4) *(LAS u32x4*)(KT + 4096 + (lane & 1) * 16 + (lane >> 1) * KC1) = z;
        CFENCE();
        u32 dw[17];
#pragma unroll
        for (int k = 0; k < 4; ++k) { u32x4 v = *(LAS u32x4*)(KT + lane * 64 + k * 16); dw[4 * k] = v.x; dw[4 * k + 1] = v.y; dw[4 * k + 2] = v.z; dw[4 * k + 3] = v.w; }
        dw[16] = *(LAS u32*)(KT + lane * 64 + 64);
#pragma unroll
        for (int k = 0; k < 4; ++k) {
          u32x4 v;
#pragma unroll
          for (int e = 0; e < 4; ++e) v[e] = __builtin_amdgcn_alignbit(dw[4 * k + e + 1], dw[4 * k + e], 16);
          *(LAS u32x4*)(KT + KC1 + lane * 64 + k * 16) = v;
        }
      }
      lds_post(sm, SY_KRDY, lane);
    }
    STAMP(3);
    tag = get_tag(sm);
    u32x4 bw[KS * 5], af[KS * 4];
    lds_wait(sm, SY_GISS, 1);
    GATHER();
    poll_flags(p.ws, fwoff, tag);
    if (hasch) {
      f32x4 y[8 - LO];
      const int par = (l15 & 1) ^ 1;
      u32 aB = (u32)(size_t)KT + (par ? KC1 : 0) + 2 * (2031 - l15 + 8 * g - par) - 3584;
      u32 bB = smb + LD_X + s * X_SZ + 16 * (28 + 2 * l15 + g);
      u32x4 fa[8], fb[8];
      lds_wait(sm, SY_XRDY, 4);
      { const CAddr c = caddr(aB, bB);
#pragma unroll
        for (int q = 0; q < 8; ++q) { fb[7 - q] = FB_(c, 7 - q); fa[q] = FA_(c, q); } }
      const u32x4 dg = diag_frag(dsk_raw, l15, g);
      SCHED();
      lds_wait(sm, SY_MISH0 + s, 1);
      PRIO(1);
      STAMP(4);
#pragma unroll
      for (int i = 0; i < 8 - LO; ++i) { f32x4 z = {0, 0, 0, 0}; y[i] = MFMA16(asb(dg), asb(fb[LO + i]), z); }
      conv_hi2(caddr(aB - 64, bB - 64), caddr(aB - 128, bB - 128), fa, fb, y, [&](int i) NOPK { if (SB1) LOAD_BW1(i); });
      conv_hi2(caddr(aB - 192, bB - 192), caddr(aB - 256, bB - 256), fa, fb, y, [&](int i) NOPK { LOAD_BW1(10 + i); if (!SB1) LOAD_BW1(i); });
      u64 fc = ld_flag(p.ws, fcoff);
      conv_hi2(caddr(aB - 320, bB - 320), caddr(aB - 384, bB - 384), fa, fb, y, [&](int i) NOPK {});
      const bool early = wall(fc == tag);
      CFENCE();
      if (early) LOAD_AF(KS, AFS); else fc = ld_flag(p.ws, fcoff);
      conv_hi2(caddr(aB - 448, bB - 448), caddr(aB - 448, bB - 448), fa, fb, y, [&](int i) NOPK {});
      STAMP(5);
      store_y<8 - LO>(rs, y, WS_YT + ch * 4096 + LO * 512, l15, g);
      lds_post(sm, SY_CONV, lane);
      DRAIN();
      SET_FLAG(WS_FD + (640 + ch) * 64);
      STAMP(6);
      if (!early) {
        if (!wall(fc == tag)) poll_flags(p.ws, fcoff, tag);
        CFENCE();
        LOAD_AF(KS, AFS);
      }
    } else {
      lds_wait(sm, SY_MISH0 + s, 1);
      PRIO(1);
      LOAD_BW();
      poll_flags(p.ws, fcoff, tag);
      LOAD_AF(KS, AFS);
    }
    lds_wait(sm, SY_G1DONE, 4);
    gemm2<true, KS>(rs, sm, wave, k0, lane, rt2, nch, bw, af, res);
  }
  if (wave >= 4) {
    STAMP(7);
    const u32 fdoff = WS_FD + ((rt2 >= 4 * LO ? 640 : 0) + ct2 * 80 + own * 20 + (lane < 20 ? lane : 0)) * 64;
    const u64 fd = ld_flag(p.ws, fdoff);
    int orow[3], och[3];
    u32x4 xres[3];
#pragma unroll
    for (int i = 0; i < 3; ++i) {
      const int id = (i < 2 || lane < 32) ? lane + 64 * i : lane;
      orow[i] = (id * 205) >> 11; och[i] = id - orow[i] * 10;
      xres[i] = *(const u32x4*)(p.x + (rt2 * 64 + own * 16 + orow[i]) * 640 + ct2 * 80 + och[i] * 8);
    }
    lds_wait(sm, SY_GPART, 4);
#pragma unroll
    for (int o = 1; o < 4; ++o)
#pragma unroll
      for (int n = 0; n < 5; ++n) res[n] += *(LAS f32x4*)(sm + LD_SP + ((own - o) & 3) * 15360 + ((o - 1) * 5 + n) * 1024 + l16);
    if (!wall(fd == tag)) poll_n(p.ws, fdoff, lane < 20, tag);
    CFENCE();
    STAMP(12);
    const u32 yo = (lane >> 3) * 4096 + (lane & 7) * 16;
    u32x4 yv[3];
#pragma unroll
    for (int i = 0; i < 3; ++i) yv[i] = BLD(yo + ((i == 2 && lane >= 32) ? OOB : 0u), WS_YT + (ct2 * 80 + own * 20 + 8 * i) * 4096 + rt2 * 128);
#pragma unroll
    for (int st = 0; st < 2; ++st) {
      const int v = (own - 1 - st) & 3;
      lds_wait(sm, SY_SP0 + v, st + 1);
#pragma unroll
      for (int n = 0; n < 5; ++n) res[n] += *(LAS f32x4*)(sm + v * 15360 + (st * 5 + n) * 1024 + l16);
    }
#pragma unroll
    for (int i = 0; i < 3; ++i) if (i < 2 || lane < 32) *(LAS u32x4*)(sm + LD_X + own * 2560 + i * 1024 + l16) = yv[i];
    lds_post(sm, SY_YST, lane);
    lds_wait(sm, SY_SP0 + ((own + 1) & 3), 3);
#pragma unroll
    for (int n = 0; n < 5; ++n) res[n] += *(LAS f32x4*)(sm + ((own + 1) & 3) * 15360 + (10 + n) * 1024 + l16);
    lds_wait(sm, SY_YST, 4);
#pragma unroll
    for (int n = 0; n < 5; ++n) {
      bf16x4 yy = trd(sm + LD_X + (n * 16 + 4 * g + (l15 >> 2)) * 128 + ((lane & 3) >> 1) * 64 + own * 16 + (lane & 1) * 8);
      u32x2 yu = __builtin_bit_cast(u32x2, yy);
      res[n] += f32x4{bflo(yu.x), bfhi(yu.x), bflo(yu.y), bfhi(yu.y)};
    }
    const __amdgpu_buffer_rsrc_t ro = __builtin_amdgcn_make_buffer_rsrc(p.out, 0, L_ * D_ * 2, 0x00020000);
    LAS char* stg = sm + LD_SP + own * 15360;
    STAMP(8);
    lds_wait(sm, SY_SP0 + own, 4);
    STAMP(9);
#pragma unroll
    for (int n = 0; n < 5; ++n) *(LAS f32x4*)(stg + l15 * 336 + n * 64 + g * 16) = res[n] + *(LAS f32x4*)(sm + LD_KT + own * 5120 + n * 1024 + l16);
    CFENCE();
#pragma unroll
    for (int i = 0; i < 3; ++i) {
      f32x4 u = *(LAS f32x4*)(stg + orow[i] * 336 + och[i] * 32), v = *(LAS f32x4*)(stg + orow[i] * 336 + och[i] * 32 + 16);
      u32x4 o = {pk2(u.x + bflo(xres[i].x), u.y + bfhi(xres[i].x)), pk2(u.z + bflo(xres[i].y), u.w + bfhi(xres[i].y)),
                 pk2(v.x + bflo(xres[i].z), v.y + bfhi(xres[i].z)), pk2(v.z + bflo(xres[i].w), v.w + bfhi(xres[i].w))};
      __builtin_amdgcn_raw_buffer_store_b128(o, ro, ((rt2 * 64 + own * 16 + orow[i]) * 640 + ct2 * 80 + och[i] * 8) * 2 + ((i < 2 || lane < 32) ? 0u : OOB), 0, 16);
    }
  }
  STAMP(10);
  if (wave == EW) while (__builtin_amdgcn_s_memrealtime() < t_ev + 3000) __builtin_amdgcn_s_sleep(4);
}

extern "C" void kernel_launch(void* const* d_in, const int* in_sizes, int n_in, void* d_out, int out_size,
                              void* d_ws, size_t ws_size, hipStream_t stream) {
  Params p;
  p.x = (const u16*)d_in[0]; p.Are = (const u16*)d_in[1]; p.Aim = (const u16*)d_in[2]; p.Bre = (const u16*)d_in[3];
  p.Bim = (const u16*)d_in[4]; p.Cre = (const u16*)d_in[5]; p.Cim = (const u16*)d_in[6]; p.ldt = (const u16*)d_in[7];
  p.dsk = (const u16*)d_in[8]; p.w1 = (const u16*)d_in[10]; p.b1 = (const u16*)d_in[11]; p.w2 = (const u16*)d_in[12];
  p.out = (u16*)d_out; p.ws = (char*)d_ws;
  (void)hipFuncSetAttribute((const void*)fused, hipFuncAttributeMaxDynamicSharedMemorySize, LDS_BYTES);
  fused<<<256, 512, LDS_BYTES, stream>>>(p);
}
```

```cpp
#include <hip/hip_runtime.h>
#include <stdint.h>
typedef unsigned short u16; typedef unsigned u32; typedef unsigned long long u64;
typedef __attribute__((ext_vector_type(2))) float f32x2;
typedef __attribute__((ext_vector_type(4))) float f32x4;
typedef __attribute__((ext_vector_type(16))) float f32x16;
typedef __attribute__((ext_vector_type(2))) __bf16 bf16x2;
typedef __attribute__((ext_vector_type(4))) __bf16 bf16x4;
typedef __attribute__((ext_vector_type(8))) __bf16 bf16x8;
typedef __attribute__((ext_vector_type(2))) _Float16 f16x2;
typedef __attribute__((ext_vector_type(8))) _Float16 f16x8;
typedef __attribute__((ext_vector_type(2))) u32 u32x2;
typedef __attribute__((ext_vector_type(4))) u32 u32x4;
#define LAS __attribute__((address_space(3)))
#define GAS __attribute__((address_space(1)))
#define RLX_AGENT __ATOMIC_RELAXED, __HIP_MEMORY_SCOPE_AGENT
#define RLX_WG __ATOMIC_RELAXED, __HIP_MEMORY_SCOPE_WORKGROUP
#if defined(__HIP_DEVICE_COMPILE__)
#define NOPK __attribute__((target("no-packed-fp32-ops")))
#else
#define NOPK
#endif
#define DEV __device__ __forceinline__ NOPK
#define CFENCE() asm volatile("" ::: "memory")
#define DRAIN() asm volatile("s_waitcnt vmcnt(0)" ::: "memory")
#define LDRAIN() asm volatile("s_waitcnt lgkmcnt(0)" ::: "memory")
#define VMW(n) asm volatile("s_waitcnt vmcnt(%0)" :: "i"(n) : "memory")
#define SCHED() __builtin_amdgcn_sched_barrier(0)
#define PRIO(n) __builtin_amdgcn_s_setprio(n)
constexpr int MG = 3;
constexpr int BT0 = 26, BMI = 10;
constexpr int AFG = 8;
constexpr u32 CODEPF = 43000;
constexpr int AW = 6, AR = 2;
constexpr int KG = 9, KS = 3;
constexpr int LO = 3;

constexpr int L_ = 2048, D_ = 640, F_ = 1536;
constexpr u32 WS_EPOCH = 0;
constexpr u32 WS_FT = 4096;
constexpr u32 WS_FW = WS_FT + 256 * 64;
constexpr u32 WS_FD = WS_FW + 512 * 64;
constexpr u32 WS_FC = WS_FD + 1280 * 64;
constexpr u32 WS_FA = WS_FC + 1024 * 64;
constexpr u32 WS_XNA = WS_FA + 512 * 64;
constexpr u32 WS_W2F = WS_XNA + L_ * D_ * 2;
constexpr u32 WS_ACT = WS_W2F + F_ * D_ * 2;
constexpr u32 WS_YT = WS_ACT + L_ * F_ * 2;
constexpr u32 WS_END = WS_YT + L_ * D_ * 2;
constexpr u32 OOB = 0x40000000u;
constexpr int KT_SZ = 8320, KC1 = 4160, X_SZ = 4608;
constexpr int LD_KT = 122880, LD_X = LD_KT + 3 * KT_SZ;
constexpr int LD_SYNC = LD_X + 3 * X_SZ;
constexpr int LDS_BYTES = 163840;
constexpr int LD_SP = 61440;
enum { SY_W1 = 0, SY_G1DONE, SY_TAGOK, SY_KRDY, SY_CONV, SY_MISH0, SY_MISH1, SY_MISH2, SY_MISH3, SY_XRDY, SY_GISS, SY_XT, SY_AISS,
       SY_BRDY, SY_YST, SY_SP0, SY_SP1, SY_SP2, SY_SP3, SY_FAOK, SY_RT, SY_FA0, SY_FA1, SY_LNST };
constexpr u32 SPIN_CAP = 1u << 18;

struct Params {
  const u16 *x, *Are, *Aim, *Bre, *Bim, *Cre, *Cim, *ldt, *dsk, *w1, *b1, *w2;
  u16* out; char* ws;
};

DEV float asf(u32 v) { return __builtin_bit_cast(float, v); }
DEV u32 asu(float v) { return __builtin_bit_cast(u32, v); }
DEV float bflo(u32 v) { return asf(v << 16); }
DEV float bfhi(u32 v) { return asf(v & 0xffff0000u); }
DEV u32 pk2(float a, float b) { f32x2 v = {a, b}; return __builtin_bit_cast(u32, __builtin_convertvector(v, bf16x2)); }
DEV float rbf(float a) { return bflo(pk2(a, a)); }
struct cf { float x, y; };
DEV cf cmul(cf a, cf b) { cf r = {__builtin_fmaf(-a.y, b.y, a.x * b.x), __builtin_fmaf(a.y, b.x, a.x * b.y)}; return r; }
DEV cf csq(cf a) { cf r = {__builtin_fmaf(-a.y, a.y, a.x * a.x), (a.x + a.x) * a.y}; return r; }
DEV cf cexp2r(float lr, float ph) {
  float e = __builtin_amdgcn_exp2f(lr); float f = __builtin_amdgcn_fractf(ph);
  cf r = {e * __builtin_amdgcn_cosf(f), e * __builtin_amdgcn_sinf(f)}; return r; }
DEV u32 toh(cf a) { f32x2 v = {a.x, a.y}; return __builtin_bit_cast(u32, __builtin_convertvector(v, f16x2)); }
DEV u32 tohc(cf a) { f32x2 v = {a.x, -a.y}; return __builtin_bit_cast(u32, __builtin_convertvector(v, f16x2)); }
DEV u32 hmul(u32 a_, u32 b_) {
  const f16x2 a = __builtin_bit_cast(f16x2, a_), b = __builtin_bit_cast(f16x2, b_), nb = -b;
  const f16x2 s = __builtin_shufflevector(nb, b, 1, 2);
  return __builtin_bit_cast(u32, __builtin_elementwise_fma(a.yy, s, a.xx * b)); }
DEV u32 hmulc(u32 a_, u32 b_) {
  const f16x2 a = __builtin_bit_cast(f16x2, a_), b = __builtin_bit_cast(f16x2, b_), nb = -b;
  const f16x2 s = __builtin_shufflevector(b, nb, 0, 3);
  return __builtin_bit_cast(u32, __builtin_elementwise_fma(a.yy, b.yx, a.xx * s)); }
template <int CTRL> DEV float dppf(float v) { return asf(__builtin_amdgcn_update_dpp(0, asu(v), CTRL, 0xf, 0xf, true)); }
DEV float red_mod4(float v) {
  v += dppf<0x124>(v); v += dppf<0x128>(v);
  auto r = __builtin_amdgcn_permlane16_swap(asu(v), asu(v), false, false);
  v = asf(r[0]) + asf(r[1]);
  auto q = __builtin_amdgcn_permlane32_swap(asu(v), asu(v), false, false);
  return asf(q[0]) + asf(q[1]); }
DEV float max64(float v) {
  v = __builtin_fmaxf(v, dppf<0xB1>(v)); v = __builtin_fmaxf(v, dppf<0x4E>(v)); v = __builtin_fmaxf(v, dppf<0x141>(v)); v = __builtin_fmaxf(v, dppf<0x140>(v));
  auto r = __builtin_amdgcn_permlane16_swap(asu(v), asu(v), false, false);
  v = __builtin_fmaxf(asf(r[0]), asf(r[1]));
  auto q = __builtin_amdgcn_permlane32_swap(asu(v), asu(v), false, false);
  return __builtin_fmaxf(asf(q[0]), asf(q[1])); }
DEV bool wall(bool c) { return __builtin_amdgcn_ballot_w64(c) == ~0ull; }

DEV u32 rfl(u32 v) { return (u32)__builtin_amdgcn_readfirstlane(v); }
DEV u32 lds_ld(LAS char* s, int w) { return __hip_atomic_load((LAS u32*)(s + LD_SYNC) + w, RLX_WG); }
DEV void lds_st(LAS char* s, int w, u32 v) { __hip_atomic_store((LAS u32*)(s + LD_SYNC) + w, v, RLX_WG); }
DEV void lds_wait(LAS char* s, int w, u32 want) {
#pragma unroll 1
  for (u32 n = 0; n < (1u << 22); ++n) { if (rfl(lds_ld(s, w)) >= want) break; __builtin_amdgcn_s_sleep(1); }
  CFENCE();
}
DEV void lds_wait0(LAS char* s, int w, u32 want) {
#pragma unroll 1
  for (u32 n = 0; n < (1u << 24); ++n) { if (rfl(lds_ld(s, w)) >= want) break; }
  CFENCE();
}
DEV void lds_post_nw(LAS char* s, int w, int) {
  asm volatile("s_mov_b64 exec, 1\n\tds_add_u32 %0, %1\n\ts_mov_b64 exec, -1" :: "v"((u32)(size_t)s + LD_SYNC + w * 4), "v"(1u) : "memory");
}
DEV void lds_post(LAS char* s, int w, int lane) { LDRAIN(); lds_post_nw(s, w, lane); }
DEV u32 lds_arrive(LAS char* s, int w, int lane) {
  LDRAIN();
  u32 r = 0; if (lane == 0) r = __hip_atomic_fetch_add((LAS u32*)(s + LD_SYNC) + w, 1u, RLX_WG);
  CFENCE();
  return __builtin_amdgcn_readfirstlane(r);
}
DEV u64 get_tag(LAS char* s) {
  lds_wait(s, SY_TAGOK, 1);
  return (u64)rfl(lds_ld(s, 32)) | ((u64)rfl(lds_ld(s, 33)) << 32);
}
DEV u64 ld_flag(char* ws, u32 off) { return __hip_atomic_load((GAS u64*)(ws + off), RLX_AGENT); }
DEV void poll_n(char* ws, u32 off, bool act, u64 tag) {
#pragma unroll 1
  for (u32 n = 0; n < SPIN_CAP; ++n) {
    u64 v = tag; if (act) v = ld_flag(ws, off);
    if (wall(v == tag)) break;
    __builtin_amdgcn_s_sleep(4);
  }
  CFENCE();
}
DEV void poll256(char* ws, u32 base, int lane, u64 tag) {
#pragma unroll 1
  for (u32 n = 0; n < SPIN_CAP; ++n) {
    bool ok = true;
#pragma unroll
    for (int k = 0; k < 4; ++k) ok &= ld_flag(ws, base + (lane + 64 * k) * 64) == tag;
    if (wall(ok)) break;
    __builtin_amdgcn_s_sleep(32);
  }
  CFENCE();
}
DEV bf16x4 trd(LAS char* p) { return __builtin_amdgcn_ds_read_tr16_b64_v4bf16((LAS bf16x4*)p); }
DEV bf16x8 cat(bf16x4 a, bf16x4 b) { return __builtin_shufflevector(a, b, 0, 1, 2, 3, 4, 5, 6, 7); }
DEV bf16x8 asb(u32x4 v) { return __builtin_bit_cast(bf16x8, v); }
DEV LAS char* lp(u32 a) { return (LAS char*)(size_t)a; }
#define MFMA16(a, b, c) __builtin_amdgcn_mfma_f32_16x16x32_bf16(a, b, c, 0, 0, 0)
#define MFMA32(a, b, c) __builtin_amdgcn_mfma_f32_32x32x16_bf16(a, b, c, 0, 0, 0)
#define MFMA32H(a, b, c) __builtin_amdgcn_mfma_f32_32x32x16_f16(a, b, c, 0, 0, 0)
#define BLD(voff, soff) __builtin_amdgcn_raw_buffer_load_b128(rs, (voff), (soff), 16)
#define BST(v, voff, soff) __builtin_amdgcn_raw_buffer_store_b128((v), rs, (voff) + (soff), 0, 16)

#define SET_FLAG(off) do { const u32x4 f_ = {(u32)tag, (u32)(tag >> 32), (u32)tag, (u32)(tag >> 32)}; BST(f_, l16 + (lane < 4 ? 0u : OOB), (off)); } while (0)

DEV void setm0(u32 v) { asm volatile("s_mov_b32 m0, %0\n\ts_nop 0" :: "s"(v) : "memory"); }
template <int OFF> DEV void glds(const void* sbase, u32 voff) {
  asm volatile("global_load_lds_dwordx4 %0, %1 offset:%2" :: "v"(voff), "s"(sbase), "i"(OFF) : "memory");
}
DEV u32x4 ldA(u32 a) { LAS u32* ap = (LAS u32*)lp(a); u32x4 v = {ap[0], ap[1], ap[2], ap[3]}; return v; }
DEV u32x4 ldB(u32 a) { return *(LAS u32x4*)lp(a); }
#define FA_(q, rho) ldA(ca[(q) >> 1] + 512 * (1 - ((q) & 1)) + 64 * (7 - (rho)))
#define FB_(q, rho) ldB(cb + 512 * (q) + 64 * (7 - (rho)))
template <int R> DEV void conv_hi2(const u32 (&ca)[4], u32 cb, u32x4 (&fa)[8], u32x4 (&fb)[8], f32x4 (&y)[8 - LO]) {
#pragma unroll
  for (int q = 0; q < 8; ++q) {
#pragma unroll
    for (int pp = 7 - q; pp >= 0 && pp + q >= LO; --pp) {
      y[q + pp - LO] = MFMA16(asb(fa[q]), asb(fb[pp]), y[q + pp - LO]);
      if (pp == 7 - q) { SCHED(); fb[pp] = FB_(pp, R + 1); SCHED(); }
    }
    SCHED(); fa[q] = FA_(q, R + 1); SCHED();
  }
#pragma unroll
  for (int pp = 7; pp >= 0; --pp) {
#pragma unroll
    for (int q = (pp < LO ? LO - pp : 0); q <= 7 - pp; ++q) {
      y[q + pp - LO] = MFMA16(asb(fa[q]), asb(fb[pp]), y[q + pp - LO]);
      if (R < 6 && pp == (q < LO ? LO - q : 0)) { SCHED(); fa[q] = FA_(q, R + 2); SCHED(); }
    }
    if (R < 6) { SCHED(); fb[pp] = FB_(pp, R + 2); SCHED(); }
  }
}
DEV u32x4 diag_frag(u32 dsk, int l15, int g) {
  const u32 v = (g == 2 + (l15 >> 3)) ? ((l15 & 1) ? dsk << 16 : dsk) : 0u;
  const int w = (l15 & 7) >> 1;
  u32x4 r = {w == 0 ? v : 0u, w == 1 ? v : 0u, w == 2 ? v : 0u, w == 3 ? v : 0u};
  return r;
}
template <int NT> DEV void store_y(const __amdgpu_buffer_rsrc_t rs, const f32x4 (&y)[NT], u32 base, int l15, int g) {
#pragma unroll
  for (int t = 0; t < NT; t += 2) {
    const int u = t + 1 < NT ? t + 1 : t;
    const u32 a0 = pk2(y[t].x, y[t].y), a1 = pk2(y[t].z, y[t].w), b0 = pk2(y[u].x, y[u].y), b1 = pk2(y[u].z, y[u].w);
    auto s0 = __builtin_amdgcn_permlane16_swap(a0, b0, false, false);
    auto s1 = __builtin_amdgcn_permlane16_swap(a1, b1, false, false);
    const u32x4 o = {s0[0], s1[0], s0[1], s1[1]};
    BST(o, t * 512 + ((g & 1) ? (u > t ? 512u : OOB) : 0u) + (l15 >> 2) * 128 + (g >> 1) * 64 + (l15 & 3) * 16, base);
  }
}
DEV u32 gld2(const void* sbase, u32 voff) { u32 r; asm volatile("global_load_ushort %0, %1, %2" : "=&v"(r) : "v"(voff), "s"(sbase) : "memory"); return r; }

#define LOAD_AF1(u) af[u] = BLD(l16 + (((u) % KG) & 3) * 1024, WS_ACT + ((rt2 * 4 + ((own + 1 + (u) / KG) & 3)) * 48 + k0 + (((u) % KG) & ~3)) * 1024)
DEV void gemm2g(const __amdgpu_buffer_rsrc_t rs, LAS char* sm, int own, int k0, int lane, int rt2, int nch, u32x4 (&bw)[KG * 5], u32x4 (&af)[KG * 4]) {
  const u32 l16 = lane * 16;
  LAS char* dst = sm + own * 15360 + l16;
  LAS char* rm = sm + (lane & 15) * 320 + (((lane >> 4) ^ ((lane & 15) >> 2)) * 16);
  f32x4 acc[2][5];
  u32 cv = 0;
#pragma unroll
  for (int st = 0; st < 4; ++st) {
#pragma unroll
    for (int k = 0; k < KG; ++k) {
#pragma unroll
      for (int n = 0; n < 5; ++n) {
        f32x4 z = {0, 0, 0, 0};
        acc[st & 1][n] = MFMA16(asb(bw[k * 5 + n]), asb(af[st * KG + k]), k ? acc[st & 1][n] : z);
        SCHED();
        if (n == 0 && st * KG + k + AFG < 4 * KG) LOAD_AF1(st * KG + k + AFG);
        if (st > 0 && k < 2 && n >= 2 && k * 3 + n - 2 < 5) {
          if (st < 3) *(LAS f32x4*)(dst + ((st - 1) * 5 + k * 3 + n - 2) * 1024) = acc[(st - 1) & 1][k * 3 + n - 2];
          else *(LAS f32x4*)(rm + own * 15360 + 10240 + (k * 3 + n - 2) * 64) = acc[(st - 1) & 1][k * 3 + n - 2];
        }
        if (st > 0 && k == 1 && n == 4) lds_post_nw(sm, SY_SP0 + own, lane);
        if (st == 3 && k == 3 && n == 0) cv = lds_ld(sm, SY_CONV);
        if (st == 3 && k == 6 && n == 0) { if (rfl(cv) < (u32)nch + 1) lds_wait(sm, SY_CONV, nch + 1); CFENCE(); }
        SCHED();
      }
    }
  }
#pragma unroll
  for (int n = 0; n < 5; ++n) *(LAS f32x4*)(rm + LD_KT + own * 5120 + n * 64) = acc[1][n];
  lds_post_nw(sm, SY_SP0 + own, lane);
}
DEV void gemm2s(LAS char* sm, int lane, const u32x4 (&af)[4 * KS], f32x4 (&res)[5]) {
  LAS char* src = sm + LD_SP + lane * 16;
  u32x4 bl[10];
  lds_wait(sm, SY_BRDY, 4);
#pragma unroll
  for (int q = 0; q < 10; ++q) bl[q] = *(LAS u32x4*)(src + q * 1024);
  SCHED();
#pragma unroll
  for (int k = 0; k < 4 * KS; ++k)
#pragma unroll
    for (int n = 0; n < 5; ++n) {
      const int q = k * 5 + n;
      f32x4 z = {0, 0, 0, 0};
      res[n] = MFMA16(asb(bl[q % 10]), asb(af[k]), k ? res[n] : z);
      SCHED();
      if (q + 10 < 20 * KS) bl[q % 10] = *(LAS u32x4*)(src + (q + 10) * 1024);
      SCHED();
    }
}

DEV int fresh_lane() { int l; asm volatile("v_mbcnt_lo_u32_b32 %0, -1, 0\n\tv_mbcnt_hi_u32_b32 %0, -1, %0" : "=v"(l)); return l; }
#define LANEVARS const int lane = fresh_lane(), g = lane >> 4, l15 = lane & 15, l31 = lane & 31, h = lane >> 5; const u32 l16 = lane * 16; \
  const int kf = k0 + ((lane >> 1) < nk ? (lane >> 1) : 0); \
  const u32 fcoff = WS_FC + (rt2 * 32 + (wave < 4 ? ((kf * 43) >> 7) * 2 + (lane & 1) : ((lane & 3) * 4 + 3) * 2 + (own >> 1))) * 64; \
  const u32 fwoff = WS_FW + (kf * 10 + ((ct2 * 80) >> 6) + (lane & 1)) * 64; \
  (void)g; (void)l15; (void)l31; (void)h; (void)l16; (void)fcoff; (void)fwoff

__global__ __launch_bounds__(512, 2) NOPK void fused(Params p) {
  extern __shared__ __attribute__((aligned(16))) char smem_[];
  LAS char* sm = (LAS char*)smem_;
  const u32 smb = (u32)(size_t)sm;
  const int b = __builtin_amdgcn_workgroup_id_x(), xcd = b & 7, j = b >> 3;
  const int tid = __builtin_amdgcn_workitem_id_x(), wave = __builtin_amdgcn_readfirstlane(tid >> 6), lane = tid & 63;
  const u32 l16 = lane * 16;
  const __amdgpu_buffer_rsrc_t rs = __builtin_amdgcn_make_buffer_rsrc(p.ws, 0, WS_END, 0x00020000);
  u32 cpf;
  {
    const char* pc = (const char*)(__builtin_amdgcn_s_getpc() & ~255ull);
    u32 o = (j * 8 + wave) * 256 + (lane & 1) * 128;
    if (o > CODEPF - 128) o = 0;
    asm volatile("global_load_dword %0, %1, %2" : "=&v"(cpf) : "v"(o), "s"(pc) : "memory");
  }
  asm volatile("" :: "s"(p.x), "s"(p.ws), "s"(p.w1));
  const int lnb = xcd * 32 + j;
  u32x4 xv[5];
  u64 ep;
  if (wave < 2) {
    asm volatile("global_load_dwordx2 %0, %1, %2 sc1" : "=&v"(ep) : "v"(0u), "s"(p.ws + WS_EPOCH) : "memory");
#pragma unroll
    for (int i = 0; i < 5; ++i)
      asm volatile("global_load_dwordx4 %0, %1, %2 offset:%3" : "=&v"(xv[i]) : "v"((lane & 3) * 1280 + (lane >> 2) * 16), "s"(p.x + (lnb * 8 + wave * 4) * 640), "i"(i * 256) : "memory");
  }
  const int jc = (j * 43) >> 7, jr = j - jc * 3;
  const int c0 = j < 30 ? xcd * 80 + jc * 8 + jr * 3 : 0, nch = j < 30 ? (jr < 2 ? 3 : 2) : 0;
  u32 prm[6][3], ldt_raw, dsk_raw;
  if (wave >= 4 && wave < 7) {
    const int ch = wave - 4 < nch ? c0 + wave - 4 : c0;
    const u32 po = ch * 384 + lane * 2;
    ldt_raw = gld2(p.ldt, ch * 2); dsk_raw = gld2(p.dsk, ch * 2);
#pragma unroll
    for (int it = 0; it < 3; ++it) {
      prm[0][it] = gld2(p.Are, po + it * 128); prm[1][it] = gld2(p.Aim, po + it * 128); prm[2][it] = gld2(p.Bre, po + it * 128);
      prm[3][it] = gld2(p.Bim, po + it * 128); prm[4][it] = gld2(p.Cre, po + it * 128); prm[5][it] = gld2(p.Cim, po + it * 128);
    }
  }
  if (tid < 32) lds_st(sm, tid, 0);
  LDRAIN();
  __builtin_amdgcn_s_barrier();
  SCHED();
  u64 tag;
  if (wave < 2) {
      const int gw = wave;
      PRIO(3);
      asm volatile("s_waitcnt vmcnt(0)" : "+v"(xv[0]), "+v"(xv[1]), "+v"(xv[2]), "+v"(xv[3]), "+v"(xv[4]), "+v"(ep) :: "memory");
      const bf16x2 ones = __builtin_bit_cast(bf16x2, 0x3F803F80u);
      float s2[2] = {0.f, 0.f}, q2[2] = {0.f, 0.f};
#pragma unroll
      for (int i = 0; i < 5; ++i)
#pragma unroll
        for (int k = 0; k < 4; ++k) {
          const u32 xw = xv[i][k]; const bf16x2 v = __builtin_bit_cast(bf16x2, xw);
          s2[k & 1] = __builtin_amdgcn_fdot2_f32_bf16(v, ones, s2[k & 1], false);
          q2[k & 1] = __builtin_amdgcn_fdot2_f32_bf16(v, v, q2[k & 1], false);
        }
      const float mean = red_mod4(s2[0] + s2[1]) * (1.f / 640.f);
      const float var = __builtin_fmaxf(__builtin_fmaf(-mean, mean, red_mod4(q2[0] + q2[1]) * (1.f / 640.f)), 0.f);
      const float sd = rbf(__builtin_amdgcn_sqrtf(rbf(rbf(var) + 1e-5f)));
      const float rinv = __builtin_amdgcn_rcpf(sd), nmr = -rbf(mean) * rinv;
#pragma unroll
      for (int i = 0; i < 5; ++i) {
        u32x4 o;
#pragma unroll
        for (int k = 0; k < 4; ++k) o[k] = pk2(__builtin_fmaf(bflo(xv[i][k]), rinv, nmr), __builtin_fmaf(bfhi(xv[i][k]), rinv, nmr));
        BST(o, (lane >> 2) * 128 + (lane & 3) * 16 + i * 2048, WS_XNA + lnb * 10240 + gw * 64);
      }
      lds_post_nw(sm, SY_LNST, lane);
      {
        const u64 t = ep * 0x9E3779B97F4A7C15ull + 0x632BE59BD9B4E019ull;
        tag = (u64)rfl((u32)t) | ((u64)rfl((u32)(t >> 32)) << 32);
        if (tid == 0) { const u32x4 tv = {(u32)tag, (u32)(tag >> 32), (u32)ep, (u32)(ep >> 32)}; *(LAS u32x4*)(sm + LD_SYNC + 128) = tv; CFENCE(); lds_st(sm, SY_TAGOK, 1); }
      }
      DRAIN();
      SET_FLAG(WS_FA + (lnb * 2 + gw) * 64);
      lds_post_nw(sm, SY_FAOK, lane);
      if (lds_arrive(sm, SY_XT, lane) == 1) SET_FLAG(WS_FT + lnb * 64);
  }
  const int rp = (xcd & 1) * 8 + (j & 7), cs = (xcd >> 1) * 4 + (j >> 3), ri = (j & 7) + 2 * (j >> 3);
  if (wave == 7) {
    PRIO(3);
    u32 so[3];
#pragma unroll
    for (int r = 0; r < 3; ++r) { const int c = r * 64 + lane, row = c / 12, cc = c - row * 12; so[r] = row * 3072 + cc * 16 + 2048 - 1024 * r; }
#pragma unroll 1
    for (int bl = 0; bl < 8; ++bl) {
      if (bl == 1) lds_wait(sm, SY_FAOK, 2);
      const char* src = (const char*)p.w1 + cs * 192 - 2048 + ((bl + ri) & 7) * (80 * 3072);
#pragma unroll
      for (int q = 0; q < 5; ++q) {
        setm0(smb + bl * 15360 + q * 3072);
        glds<0>(src + q * 49152, so[0]); glds<1024>(src + q * 49152, so[1]); glds<2048>(src + q * 49152, so[2]);
      }
      VMW(30);
      if (bl >= 2 && lane == 0) lds_st(sm, SY_W1, 5 * (bl - 1));
    }
    u64 ft[4];
#pragma unroll
    for (int k = 0; k < 4; ++k) asm volatile("global_load_dwordx2 %0, %1, %2 sc1" : "=&v"(ft[k]) : "v"((u32)(lane + 64 * k) * 64), "s"(p.ws + WS_FT) : "memory");
    VMW(19);
    if (lane == 0) lds_st(sm, SY_W1, 35);
    VMW(4);
    if (lane == 0) lds_st(sm, SY_W1, 40);
    tag = get_tag(sm);
    asm volatile("s_waitcnt vmcnt(0)" : "+v"(ft[0]), "+v"(ft[1]), "+v"(ft[2]), "+v"(ft[3]) :: "memory");
    if (!wall(ft[0] == tag && ft[1] == tag && ft[2] == tag && ft[3] == tag)) poll256(p.ws, WS_FT, lane, tag);
    CFENCE();
    lds_wait(sm, SY_RT, 2);
    lds_post_nw(sm, SY_GISS, lane);
  }
  const int rt2 = xcd * 4 + (j & 3), ct2 = j >> 2;
  const int own = wave & 3;
  const int rot = 5 * (ri & 7);
  const int kq = (wave + j + (j >> 2)) & 3;
  const int k0 = kq * 12 + (wave < 4 ? 0 : KG), nk = wave < 4 ? KG : KS;
  f32x4 res[5];
#define LOAD_BW1(q) bw[q] = BLD(l16 + (((q) % 5) & 3) * 1024, WS_W2F + ((k0 + (q) / 5) * 40 + ct2 * 5 + (((q) % 5) & 4)) * 1024)
#define LOAD_BW() do { _Pragma("unroll") for (int q = 0; q < KS * 5; ++q) LOAD_BW1(q); SCHED(); } while (0)
#define LOAD_AFS() do { _Pragma("unroll") for (int u = 0; u < 4 * KS; ++u) \
    af[u] = BLD(l16 + (u % KS) * 1024, WS_ACT + ((rt2 * 4 + own) * 48 + (u / KS) * 12 + KG) * 1024); SCHED(); } while (0)
#define PUT_B() do { lds_wait(sm, SY_G1DONE, 4); \
    _Pragma("unroll") for (int q = 0; q < KS * 5; ++q) *(LAS u32x4*)(sm + LD_SP + kq * 15360 + q * 1024 + l16) = bw[q]; \
    lds_post_nw(sm, SY_BRDY, lane); } while (0)
#define GATHER_LD() do { PRIO(3); \
      const u32 go = (lane >> 3) * 10240 + (xcd * 10 + jc) * 128 + (lane & 7) * 16 + jr * 4 + (nch ? 0u : OOB); \
      _Pragma("unroll") for (int it = 0; it < 8; ++it) gv[it] = __builtin_amdgcn_raw_buffer_load_b64(rs, go, WS_XNA + (own * 8 + it) * 16 * 5120, 16); \
      PRIO(0); SCHED(); } while (0)
#define GATHER_ST() do { \
      const u32 sh = jr * 16; \
      LAS u16* xd = (LAS u16*)(sm + LD_X + 480 + own * 1024 + lane * 2); \
      _Pragma("unroll") for (int it = 0; it < 8; ++it) { \
        const u64 v = (((u64)gv[it].y << 32) | gv[it].x) >> sh; \
        xd[it * 64] = (u16)v; xd[it * 64 + X_SZ / 2] = (u16)(v >> 16); xd[it * 64 + X_SZ] = (u16)(v >> 32); } \
      lds_post(sm, SY_XRDY, lane); } while (0)

  int po[3], oo[3];
  u32x4 xres[3];
#define XPRE() do { _Pragma("unroll") for (int i = 0; i < 3; ++i) { \
      const int id = (i < 2 || lane < 32) ? lane + 64 * i : lane; \
      const int orow = (id * 205) >> 11, och = id - orow * 10; \
      po[i] = orow * 320 + ((och * 2) ^ (orow >> 2)) * 16; \
      oo[i] = ((rt2 * 64 + own * 16 + orow) * 640 + ct2 * 80 + och * 8) * 2; \
      xres[i] = *(const u32x4*)((const char*)p.x + oo[i]); asm volatile("" : "+v"(po[i]), "+v"(oo[i])); } } while (0)

  if (__builtin_expect(wave < 4, 1)) {
    LANEVARS;
    const int gw = wave;
    u32x4 bw[KG * 5], af[KG * 4];
    u32 braw[3];
#pragma unroll
    for (int n = 0; n < 3; ++n) braw[n] = p.b1[cs * 96 + n * 32 + l31];
    if (gw >= 2) {
      PRIO(3);
      const int pid = (lnb < 240 ? lnb * 2 : 0) + (gw - 2), rks = pid / 10, cb = pid - rks * 10;
      const char* src = (const char*)p.w2 + (32 * rks * 640 + 64 * cb) * 2;
      const u32 wo = (lane >> 3) * 1280 + (lane & 7) * 16;
      lds_wait(sm, SY_LNST, 2);
      setm0(smb + LD_X + (gw - 2) * 4096);
      glds<0>(src, wo); glds<1024>(src + 9216, wo); glds<2048>(src + 18432, wo); glds<3072>(src + 27648, wo);
      DRAIN();
      u32x4 wr[4];
#pragma unroll
      for (int n = 0; n < 4; ++n) {
        LAS char* tp = sm + LD_X + (gw - 2) * 4096 + (16 * (g >> 1) + 4 * (g & 1) + (l15 >> 2)) * 128 + 32 * n + 8 * (lane & 3);
        wr[n] = __builtin_bit_cast(u32x4, cat(trd(tp), trd(tp + 1024)));
      }
      lds_post(sm, SY_RT, lane);
#pragma unroll
      for (int n = 0; n < 4; ++n) BST(wr[n], l16 + n * 1024 + (lnb < 240 ? 0u : OOB), WS_W2F + (rks * 40 + 4 * cb) * 1024);
      tag = get_tag(sm);
      DRAIN();
      if (lnb < 240) SET_FLAG(WS_FW + pid * 64);
    }
    if (gw < 2) {
      poll_n(p.ws, WS_FA + (rp * 32 + gw * 8 + (lane & 8) * 2 + (lane & 7)) * 64, lane < 16, tag);
      lds_post_nw(sm, SY_FA0 + gw, lane);
    } else lds_wait(sm, SY_FA0 + (gw & 1), 1);
    asm volatile("" :: "v"(cpf));
    PRIO(2);
    u32x4 a[40];
    const u32 av = (l31 >> 3) * 10240 + h * 128 + (l31 & 7) * 16;
#define LOAD_A(t) a[t] = BLD(av + ((t) % 5) * 256, WS_XNA + (rp * 32 + gw * 8) * 5120 + (((t) / 5 * 5 + rot >= 40) ? (t) / 5 * 5 + rot - 40 : (t) / 5 * 5 + rot) * 256)
#pragma unroll
    for (int t = 0; t < AW; ++t) LOAD_A(t);
    u64 fw;
    SCHED();
    lds_wait(sm, SY_W1, 5);
    f32x16 acc[3];
    {
      u32 wb[2], wp = 0;
      wb[0] = smb + 192 * (8 * h + (l15 >> 2)) + 32 * (g & 1) + 8 * (lane & 3); wb[1] = wb[0] + 20 * 3072;
      asm("" : "+v"(wb[0]), "+v"(wb[1]));
      bf16x8 wf[3][3];
#define RDW(t, n) wf[(t) % 3][n] = cat(trd(lp(wb[(t) / 20] + ((t) % 20) * 3072 + (n) * 64)), trd(lp(wb[(t) / 20] + ((t) % 20) * 3072 + (n) * 64 + 768)))
#pragma unroll
      for (int t = 0; t < 2; ++t) { RDW(t, 0); RDW(t, 1); RDW(t, 2); }
      const u32x4 xe = {h ? 0u : 0x3F80u, 0u, 0u, 0u};
#pragma unroll
      for (int n = 0; n < 3; ++n) {
        f32x16 z;
#pragma unroll
        for (int i = 0; i < 16; ++i) z[i] = 0.f;
        const u32x4 we = {braw[n], 0u, 0u, 0u};
        acc[n] = MFMA32(asb(we), asb(xe), z);
      }
      SCHED();
#pragma unroll
      for (int t5 = 0; t5 < 40; t5 += 5)
#pragma unroll
      for (int t = t5; t < t5 + 5; ++t) {
#pragma unroll
        for (int n = 0; n < 3; ++n) {
          acc[n] = MFMA32(wf[t % 3][n], asb(a[t]), acc[n]);
          SCHED();
          if ((t + 2) % 5 == 0 && n == 0 && t + 2 < 40) {
            const u32 need = t + 7;
            if (rfl(wp) < need) lds_wait(sm, SY_W1, need);
            CFENCE();
          }
          if (t + 2 < 40) RDW(t + 2, n);
          if ((t + 3) % 5 == 0 && n == 0) wp = lds_ld(sm, SY_W1);
          if (n < AR && AW + t * AR + n < 40) LOAD_A(AW + t * AR + n);
          if (n == 2 && t == 24) lds_post_nw(sm, SY_AISS, lane);
          if (t == BT0 - 8 && n == 2) fw = ld_flag(p.ws, fwoff);
          if (t == BT0 - 1 && n == 2) { if (!wall(fw == tag)) poll_n(p.ws, fwoff, lane < 2 * nk, tag); CFENCE(); }
          if (t >= BT0 && n == 1) LOAD_BW1(t - BT0);
          SCHED();
        }
      }
    }
    lds_post_nw(sm, SY_G1DONE, lane);
    {
      const u32 ao = (l31 >> 4) * 48 * 1024 + (h * 16 + l15) * 16;
#pragma unroll
      for (int n = 0; n < 3; ++n)
#pragma unroll
        for (int c2 = 0; c2 < 2; ++c2) {
          float mv[8];
          if (n * 2 + c2 == MG) lds_post_nw(sm, SY_MISH0 + gw, lane);
#pragma unroll
          for (int e = 0; e < 8; ++e) {
            const float hv = acc[n][8 * c2 + e];
            const float ex = __builtin_amdgcn_exp2f(hv * 1.44269504089f);
            const float rc = __builtin_amdgcn_rcpf(__builtin_fmaf(ex, ex + 2.f, 2.f));
            mv[e] = hv * __builtin_fmaf(rc, -2.f, 1.f);
            if ((e & 3) == 3) {
              const int q = (n * 2 + c2) * 4 + (e >> 2) * 2;
              if (q < BMI) { SCHED(); LOAD_BW1(40 - BT0 + q); LOAD_BW1(40 - BT0 + q + 1); SCHED(); }
            }
          }
          u32x4 o = {pk2(mv[0], mv[1]), pk2(mv[2], mv[3]), pk2(mv[4], mv[5]), pk2(mv[6], mv[7])};
          BST(o, ao + n * 1024 + c2 * 512, WS_ACT + ((rp * 8 + gw * 2) * 48 + cs * 3) * 1024);
        }
    }
    DRAIN();
    SET_FLAG(WS_FC + ((rp * 2 + (gw >> 1)) * 32 + cs * 2 + (gw & 1)) * 64);
    PRIO(0);
#pragma unroll
    for (int q = 40 - BT0 + BMI; q < KG * 5; ++q) { LOAD_BW1(q); VMW(16); }
    poll_n(p.ws, fcoff, lane < 2 * KG, tag);
#pragma unroll
    for (int u = 0; u < AFG; ++u) LOAD_AF1(u);
    SCHED();
    lds_wait(sm, SY_G1DONE, 4);
    gemm2g(rs, sm, own, k0, fresh_lane(), rt2, nch, bw, af);
  } else if (wave == 7) {
    LANEVARS;
    const u32 dsk3 = p.dsk[c0 + (lane < nch ? lane : 0)];
    asm volatile("" :: "v"(cpf));
    if (b == 0 && lane == 0) __hip_atomic_store((GAS u64*)(p.ws + WS_EPOCH), ((u64)lds_ld(sm, 34) | ((u64)lds_ld(sm, 35) << 32)) + 1, RLX_AGENT);
    PRIO(0);
    if (lane < 30) {
      const u32x4 z = {0, 0, 0, 0};
#pragma unroll
      for (int c = 0; c < 3; ++c) *(LAS u32x4*)(sm + LD_X + c * X_SZ + l16) = z;
    }
    u32x4 bw[KS * 5], af[KS * 4];
    u32x2 gv[8];
    GATHER_LD();
    GATHER_ST();
    poll_n(p.ws, fwoff, lane < 2 * nk, tag);
    lds_wait(sm, SY_AISS, 4);
    LOAD_BW();
    lds_wait(sm, SY_KRDY, nch);
    lds_wait(sm, SY_XRDY, 4);
    const int par = (l15 & 1) ^ 1;
    u32 ha[3][2], hb = smb + LD_X + 16 * (28 + 2 * l15 + g) - 448;
#pragma unroll
    for (int c = 0; c < 3; ++c) {
      ha[c][0] = smb + LD_KT + c * KT_SZ + (par ? KC1 : 0) + 2 * (2031 - l15 + 8 * g - par) - 512 - 448; ha[c][1] = ha[c][0] - 1024;
      asm("" : "+v"(ha[c][0]), "+v"(ha[c][1]));
    }
    asm("" : "+v"(hb));
#define HA(c, q, rho) ldA(ha[c][(q) >> 1] + 512 * (1 - ((q) & 1)) + 64 * (7 - (rho)))
#define HB(c, q, rho) ldB(hb + (c) * X_SZ + 512 * (q) + 64 * (7 - (rho)))
    u32x4 fa[3][LO], fb[3][LO];
    f32x4 y[3][LO];
#pragma unroll
    for (int c = 0; c < 3; ++c)
#pragma unroll
      for (int q = 0; q < LO; ++q) { fa[c][q] = HA(c, q, 0); fb[c][q] = HB(c, q, 0); }
    lds_wait(sm, SY_MISH3, 1);
    PRIO(1);
#pragma unroll
    for (int c = 0; c < 3; ++c) {
      const u32x4 dg = diag_frag(__builtin_amdgcn_readlane(dsk3, c), l15, g);
#pragma unroll
      for (int i = 0; i < LO; ++i) { f32x4 z = {0, 0, 0, 0}; y[c][i] = MFMA16(asb(dg), asb(fb[c][i]), z); }
    }
    SCHED();
#define HSTEP(rho) do { _Pragma("unroll") for (int c = 0; c < 3; ++c) _Pragma("unroll") for (int q = 0; q < LO; ++q) { \
      _Pragma("unroll") for (int pp = LO - 1 - q; pp >= 0; --pp) { \
        y[c][q + pp] = MFMA16(asb(fa[c][q]), asb(fb[c][pp]), y[c][q + pp]); \
        if (pp == LO - 1 - q && (rho) < 7) { SCHED(); fb[c][pp] = HB(c, pp, (rho) + 1); SCHED(); } } \
      if ((rho) < 7) { SCHED(); fa[c][q] = HA(c, q, (rho) + 1); SCHED(); } } } while (0)
    HSTEP(0); HSTEP(1); HSTEP(2); HSTEP(3); HSTEP(4); HSTEP(5);
    HSTEP(6); HSTEP(7);
    PRIO(3);
#pragma unroll
    for (int c = 0; c < 3; ++c) store_y<LO>(rs, y[c], c < nch ? WS_YT + (c0 + c) * 4096 : OOB, l15, g);
    const u64 fc = ld_flag(p.ws, fcoff);
    lds_post(sm, SY_CONV, lane);
    PUT_B();
    DRAIN();
    const bool early = wall(fc == tag);
    CFENCE();
    { const u32x4 f_ = {(u32)tag, (u32)(tag >> 32), (u32)tag, (u32)(tag >> 32)}; BST(f_, l16 + (lane < 4 * nch ? 0u : OOB), WS_FD + c0 * 64); }
    if (early) LOAD_AFS();
    PRIO(1);
    if (!early) { poll_n(p.ws, fcoff, lane < 4, tag); LOAD_AFS(); }
    XPRE();
    gemm2s(sm, lane, af, res);
  } else {
    LANEVARS;
    const int s = wave - 4;
    const bool hasch = s < nch;
    const int ch = hasch ? c0 + s : c0;
    LAS char* KT = sm + LD_KT + s * KT_SZ;
    asm volatile("s_waitcnt vmcnt(0)" : "+v"(ldt_raw), "+v"(dsk_raw) :: "memory");
    asm volatile("" :: "v"(cpf));
#pragma unroll
    for (int k = 0; k < 6; ++k) asm volatile("" : "+v"(prm[k][0]), "+v"(prm[k][1]), "+v"(prm[k][2]));
    SCHED();
    u32x2 gv[8];
    bool gearly = false;
    if (hasch) {
      const int m = l31;
      const float dt = rbf(__builtin_amdgcn_exp2f(bflo(ldt_raw) * 1.44269504089f));
      cf cc[3]; float mx = 0.f;
#pragma unroll
      for (int it = 0; it < 3; ++it) {
        const cf Bc = {bflo(prm[2][it]), bflo(prm[3][it])}, Cc = {bflo(prm[4][it]), bflo(prm[5][it])};
        cc[it] = cmul(Cc, Bc);
        mx = __builtin_fmaxf(mx, __builtin_fmaxf(__builtin_fabsf(cc[it].x), __builtin_fabsf(cc[it].y)));
      }
      const int ex = __builtin_amdgcn_frexp_expf(max64(mx));
      const float ksc = __builtin_amdgcn_ldexpf(dt, ex - 10);
      f32x16 kacc;
#pragma unroll
      for (int i = 0; i < 16; ++i) kacc[i] = 0.f;
      const int baseA = 512 * h + 16 * ((m >> 2) ^ (h << 1)) + 4 * (m & 3);
      const int baseB = 4096 + 2048 * h + 16 * (m >> 2) + 4 * (m & 3);
      const int rdA = m * 128, rsw = (m >> 1) & 7;
      u32x4 ta[4], tb[4];
#define KMFMA(ks) kacc = MFMA32H(__builtin_bit_cast(f16x8, ta[ks]), __builtin_bit_cast(f16x8, tb[ks]), kacc)
#define WRA(rr, v) (*(LAS u32*)(KT + ((baseA ^ (16 * ((((rr) & 3) >> 1) | ((((rr) >> 2) & 1) << 2)))) + 128 * (((rr) & 3) + 8 * ((rr) >> 2)))) = (v))
#define WRB(be, v) (*(LAS u32*)(KT + ((baseB ^ (16 * ((be) >> 1))) + 128 * (be))) = (v))
#define SWAP(dst, a0, a1) do { auto q_ = __builtin_amdgcn_permlane32_swap((a0), (a1), false, false); dst[0] = q_[0]; dst[1] = q_[1]; } while (0)
#pragma unroll
      for (int it = 0; it < 3; ++it) {
        const float lr = dt * bflo(prm[0][it]) * 1.44269504089f, ph = dt * bflo(prm[1][it]) * 0.15915494309f;
        const cf c = {__builtin_amdgcn_ldexpf(cc[it].x, 10 - ex), __builtin_amdgcn_ldexpf(cc[it].y, 10 - ex)};
        const cf e1 = cexp2r(lr, ph), e128 = cexp2r(lr * 128.f, ph * 128.f);
        const cf e2 = csq(e1), e256 = csq(e128), e4 = csq(e2), e512 = csq(e256);
        SCHED(); if (it) KMFMA(0); SCHED();
        const cf e8 = csq(e4), e1024 = csq(e512), e16 = csq(e8), e32 = csq(e16);
        SCHED(); if (it) KMFMA(1); SCHED();
        const cf e64 = csq(e32), c512 = cmul(c, e512), c1024 = cmul(c, e1024);
        const u32 g1 = toh(e1), g2 = toh(e2), g4 = toh(e4), g8 = toh(e8), g16 = toh(e16);
        SCHED(); if (it) KMFMA(2); SCHED();
        const u32 g256 = toh(e256), g512 = toh(e512), k32 = tohc(e32), k64 = tohc(e64), k128 = tohc(e128);
        const u32 g3 = hmul(g2, g1), g24 = hmul(g16, g8), k96 = hmul(k64, k32);
        const u32 b0 = toh(c), b7 = toh(c512), b10 = toh(c1024);
        SCHED(); if (it) KMFMA(3); SCHED();
        u32 R0[2], R7[2], R10[2], P[4][2], H1[2], H2[2], H3[2], H4[2], H8[2], H16[2], H24[2], H256[2];
        SWAP(R0, b0, hmul(b0, g4)); SWAP(R7, b7, hmul(b7, g256)); SWAP(R10, b10, hmul(b10, g512));
        SWAP(P[0], 0x00003C00u, k128); SWAP(P[1], k32, hmul(k128, k32)); SWAP(P[2], k64, hmul(k128, k64)); SWAP(P[3], k96, hmul(k128, k96));
        SWAP(H1, g1, g1); SWAP(H2, g2, g2); SWAP(H3, g3, g3); SWAP(H4, g4, g4); SWAP(H8, g8, g8); SWAP(H16, g16, g16); SWAP(H24, g24, g24); SWAP(H256, g256, g256);
        SCHED();
#pragma unroll
        for (int sp = 0; sp < 2; ++sp) {
#pragma unroll
          for (int a = 0; a < 4; ++a) {
            WRB(4 * a, P[a][sp]); WRB(4 * a + 1, hmulc(P[a][sp], H8[sp])); SCHED();
            WRB(4 * a + 2, hmulc(P[a][sp], H16[sp])); WRB(4 * a + 3, hmulc(P[a][sp], H24[sp])); SCHED();
            if (sp) { KMFMA(a); SCHED(); }
          }
          const u32 r4 = hmul(R0[sp], H256[sp]), r12 = hmul(R10[sp], H256[sp]);
          WRA(0, R0[sp]); WRA(1, hmul(R0[sp], H1[sp])); SCHED();
          WRA(2, hmul(R0[sp], H2[sp])); WRA(3, hmul(R0[sp], H3[sp])); SCHED();
          WRA(4, r4); WRA(5, hmul(r4, H2[sp])); SCHED();
          WRA(6, hmul(r4, H4[sp])); WRA(7, R7[sp]); SCHED();
          WRA(8, hmul(R7[sp], H4[sp])); WRA(9, hmul(R7[sp], H8[sp])); SCHED();
          WRA(10, R10[sp]); WRA(11, hmul(R10[sp], H8[sp])); SCHED();
          WRA(12, r12); WRA(13, hmul(r12, H8[sp]));
          CFENCE(); SCHED();
#pragma unroll
          for (int ks = 0; ks < 4; ++ks) {
            const int so = rdA + 16 * ((2 * ks + h) ^ rsw);
            ta[ks] = *(LAS u32x4*)(KT + so); tb[ks] = *(LAS u32x4*)(KT + 4096 + so);
          }
          CFENCE(); SCHED();
        }
      }
      KMFMA(0); KMFMA(1); KMFMA(2); KMFMA(3);
      CFENCE();
      gearly = rfl(lds_ld(sm, SY_GISS)) != 0;
      CFENCE();
      if (gearly) GATHER_LD();
      {
        const int bb = m;
        float kv[14];
#pragma unroll
        for (int i = 0; i < 14; ++i) kv[i] = kacc[i] * ksc;
        u32x2 s0 = {pk2(kv[3], kv[2]), pk2(kv[1], kv[0])};
        *(LAS u32x2*)(KT + 2 * (2044 - 8 * bb - 4 * h)) = s0;
        u32x2 s1 = {pk2(kv[6], kv[5]), pk2(kv[4], kv[4])};
        *(LAS u32x2*)(KT + 2 * (2044 - 256 - 8 * bb - 4 * h)) = s1;
        u32x4 s2 = {pk2(kv[9], kv[9]), pk2(kv[8], kv[8]), pk2(kv[8], kv[7]), pk2(kv[7], kv[7])};
        *(LAS u32x4*)(KT + 2 * (2040 - 512 - 256 * h - 8 * bb)) = s2;
#pragma unroll
        for (int a2 = 0; a2 < 2; ++a2) {
          const float v0 = kv[10 + 2 * a2], v8 = kv[11 + 2 * a2], tie = (bb & 1) ? v8 : v0;
          u32x4 s3 = {pk2(v8, v8), pk2(v8, tie), pk2(v0, v0), pk2(v0, v0)};
          *(LAS u32x4*)(KT + 2 * (2040 - 1024 - 512 * h - 256 * a2 - 8 * bb)) = s3;
        }
        u32x4 z = {0, 0, 0, 0};
        if (lane < 4) *(LAS u32x4*)(KT + 4096 + (lane & 1) * 16 + (lane >> 1) * KC1) = z;
        CFENCE();
        u32 dw[17];
#pragma unroll
        for (int k = 0; k < 4; ++k) { u32x4 v = *(LAS u32x4*)(KT + lane * 64 + k * 16); dw[4 * k] = v.x; dw[4 * k + 1] = v.y; dw[4 * k + 2] = v.z; dw[4 * k + 3] = v.w; }
        dw[16] = *(LAS u32*)(KT + lane * 64 + 64);
#pragma unroll
        for (int k = 0; k < 4; ++k) {
          u32x4 v;
#pragma unroll
          for (int e = 0; e < 4; ++e) v[e] = __builtin_amdgcn_alignbit(dw[4 * k + e + 1], dw[4 * k + e], 16);
          *(LAS u32x4*)(KT + KC1 + lane * 64 + k * 16) = v;
        }
      }
      lds_post(sm, SY_KRDY, lane);
    }
    tag = get_tag(sm);
    u32x4 bw[KS * 5], af[KS * 4];
    if (!gearly) { lds_wait(sm, SY_GISS, 1); GATHER_LD(); }
    const u64 fw = ld_flag(p.ws, fwoff);
    GATHER_ST();
    if (!wall(fw == tag)) poll_n(p.ws, fwoff, lane < 2 * nk, tag);
    CFENCE();
    LOAD_BW();
    f32x4 y[8 - LO];
#pragma unroll
    for (int i = 0; i < 8 - LO; ++i) { f32x4 z = {0, 0, 0, 0}; y[i] = z; }
    if (hasch) {
      const int par = (l15 & 1) ^ 1;
      u32 ca[4], cb = smb + LD_X + s * X_SZ + 16 * (28 + 2 * l15 + g) - 448;
#pragma unroll
      for (int i = 0; i < 4; ++i) { ca[i] = (u32)(size_t)KT + (par ? KC1 : 0) + 2 * (2031 - l15 + 8 * g - par) - 512 - 448 - 1024 * i; asm("" : "+v"(ca[i])); }
      asm("" : "+v"(cb));
      u32x4 fa[8], fb[8];
      lds_wait(sm, SY_XRDY, 4);
#pragma unroll
      for (int q = 0; q < 8; ++q) { fb[7 - q] = FB_(7 - q, 0); fa[q] = FA_(q, 0); }
      u32x4 dg = diag_frag(dsk_raw, l15, g);
      asm volatile("" : "+v"(dg));
      lds_wait(sm, SY_MISH0 + s, 1);
      PRIO(1);
#pragma unroll
      for (int i = 0; i < 8 - LO; ++i) y[i] = MFMA16(asb(dg), asb(fb[LO + i]), y[i]);
      conv_hi2<0>(ca, cb, fa, fb, y);
      conv_hi2<2>(ca, cb, fa, fb, y);
      conv_hi2<4>(ca, cb, fa, fb, y);
      conv_hi2<6>(ca, cb, fa, fb, y);
      lds_post(sm, SY_CONV, lane);
    } else lds_wait(sm, SY_MISH0 + s, 1);
    PRIO(3);
    store_y<8 - LO>(rs, y, hasch ? WS_YT + ch * 4096 + LO * 512 : OOB, l15, g);
    const u64 fc = ld_flag(p.ws, fcoff);
    PUT_B();
    DRAIN();
    const bool early = wall(fc == tag);
    CFENCE();
    SET_FLAG(hasch ? WS_FD + (640 + ch) * 64 : OOB);
    if (early) LOAD_AFS();
    PRIO(1);
    if (!early) { poll_n(p.ws, fcoff, lane < 4, tag); LOAD_AFS(); }
    XPRE();
    gemm2s(sm, lane, af, res);
  }
  if (wave >= 4) {
    LANEVARS;
#define PIN5(r) asm volatile("" : "+v"(r[0]), "+v"(r[1]), "+v"(r[2]), "+v"(r[3]), "+v"(r[4]))
#define PINUV() asm volatile("" : "+v"(u[0]), "+v"(u[1]), "+v"(u[2]), "+v"(v[0]), "+v"(v[1]), "+v"(v[2]))
#define RED_G(st) do { const int w_ = (own - 1 - (st)) & 3; lds_wait0(sm, SY_SP0 + w_, (st) + 1); f32x4 t_[5]; \
      _Pragma("unroll") for (int n = 0; n < 5; ++n) t_[n] = *(LAS f32x4*)(sm + w_ * 15360 + ((st) * 5 + n) * 1024 + l16); \
      SCHED(); \
      _Pragma("unroll") for (int n = 0; n < 5; ++n) res[n] += t_[n]; \
      PIN5(res); SCHED(); } while (0)
    const u32 fd0 = WS_FD + ((rt2 >= 4 * LO ? 640 : 0) + ct2 * 80 + own * 20 + (lane < 20 ? lane : 0)) * 64;
    const u64 f0 = ld_flag(p.ws, fd0);
    RED_G(0);
    f32x4 u[3], v[3];
#pragma unroll
    for (int i = 0; i < 3; ++i) {
      u[i] = f32x4{bflo(xres[i].x), bfhi(xres[i].x), bflo(xres[i].y), bfhi(xres[i].y)};
      v[i] = f32x4{bflo(xres[i].z), bfhi(xres[i].z), bflo(xres[i].w), bfhi(xres[i].w)};
    }
    PINUV();
#define ADD_P(base) do { f32x4 la[3], lb[3]; \
      _Pragma("unroll") for (int i = 0; i < 3; ++i) { la[i] = *(LAS f32x4*)((base) + po[i]); lb[i] = *(LAS f32x4*)((base) + (po[i] ^ 16)); } \
      _Pragma("unroll") for (int i = 0; i < 3; ++i) { u[i] += la[i]; v[i] += lb[i]; } } while (0)
    SCHED();
    if (!wall(f0 == tag)) poll_n(p.ws, fd0, lane < 20, tag);
    CFENCE();
    u32x4 yv[3];
#pragma unroll
    for (int i = 0; i < 3; ++i)
      yv[i] = BLD((lane >> 3) * 4096 + (lane & 7) * 16 + ((i == 2 && lane >= 32) ? OOB : 0u), WS_YT + (ct2 * 80 + own * 20 + 8 * i) * 4096 + rt2 * 128);
    RED_G(1);
#pragma unroll
    for (int i = 0; i < 3; ++i) if (i < 2 || lane < 32) *(LAS u32x4*)(sm + LD_X + own * 2560 + i * 1024 + l16) = yv[i];
    lds_post_nw(sm, SY_YST, lane);
    lds_wait0(sm, SY_SP0 + ((own + 1) & 3), 3);
    ADD_P(sm + ((own + 1) & 3) * 15360 + 10240);
    PINUV();
    u32 ya = smb + LD_X + (4 * g + (l15 >> 2)) * 128 + ((lane & 3) >> 1) * 64 + own * 16 + (lane & 1) * 8;
    u32 wa = smb + ((own - 1) & 3) * 15360 + l15 * 320 + (g ^ (l15 >> 2)) * 16;
    u32 ra[6], rb[6];
#pragma unroll
    for (int i = 0; i < 6; ++i) {
      const u32 o = po[i >> 1] ^ ((i & 1) * 16);
      ra[i] = smb + ((own - 1) & 3) * 15360 + o; rb[i] = smb + LD_KT + own * 5120 + o;
      asm volatile("" : "+v"(ra[i]), "+v"(rb[i]));
    }
    u32 o2 = oo[2] + (lane < 32 ? 0u : OOB);
    asm volatile("" : "+v"(ya), "+v"(wa), "+v"(o2));
    lds_wait0(sm, SY_YST, 4);
    bf16x4 yy[5];
#pragma unroll
    for (int n = 0; n < 5; ++n) yy[n] = trd(lp(ya + n * 2048));
    const u32 lastc = lds_ld(sm, SY_SP0 + own);
    CFENCE();
#pragma unroll
    for (int n = 0; n < 5; ++n) {
      const u32x2 yu = __builtin_bit_cast(u32x2, yy[n]);
      *(LAS f32x4*)lp(wa + n * 64) = res[n] + f32x4{bflo(yu.x), bfhi(yu.x), bflo(yu.y), bfhi(yu.y)};
    }
    CFENCE();
    f32x4 sa[3], sb[3], la[3], lb[3];
#pragma unroll
    for (int i = 0; i < 3; ++i) { sa[i] = *(LAS f32x4*)lp(ra[2 * i]); sb[i] = *(LAS f32x4*)lp(ra[2 * i + 1]); }
    const __amdgpu_buffer_rsrc_t ro = __builtin_amdgcn_make_buffer_rsrc(p.out, 0, L_ * D_ * 2, 0x00020000);
    if (rfl(lastc) < 4) lds_wait0(sm, SY_SP0 + own, 4);
    CFENCE();
#pragma unroll
    for (int i = 0; i < 3; ++i) { la[i] = *(LAS f32x4*)lp(rb[2 * i]); lb[i] = *(LAS f32x4*)lp(rb[2 * i + 1]); }
#pragma unroll
    for (int i = 0; i < 3; ++i) { u[i] = (u[i] + sa[i]) + la[i]; v[i] = (v[i] + sb[i]) + lb[i]; }
#pragma unroll
    for (int i = 0; i < 3; ++i) {
      const u32x4 o = {pk2(u[i].x, u[i].y), pk2(u[i].z, u[i].w), pk2(v[i].x, v[i].y), pk2(v[i].z, v[i].w)};
      __builtin_amdgcn_raw_buffer_store_b128(o, ro, i < 2 ? oo[i] : o2, 0, 16);
    }
  }
}

extern "C" void kernel_launch(void* const* d_in, const int* in_sizes, int n_in, void* d_out, int out_size,
                              void* d_ws, size_t ws_size, hipStream_t stream) {
  Params p;
  p.x = (const u16*)d_in[0]; p.Are = (const u16*)d_in[1]; p.Aim = (const u16*)d_in[2]; p.Bre = (const u16*)d_in[3];
  p.Bim = (const u16*)d_in[4]; p.Cre = (const u16*)d_in[5]; p.Cim = (const u16*)d_in[6]; p.ldt = (const u16*)d_in[7];
  p.dsk = (const u16*)d_in[8]; p.w1 = (const u16*)d_in[10]; p.b1 = (const u16*)d_in[11]; p.w2 = (const u16*)d_in[12];
  p.out = (u16*)d_out; p.ws = (char*)d_ws;
  (void)hipFuncSetAttribute((const void*)fused, hipFuncAttributeMaxDynamicSharedMemorySize, LDS_BYTES);
  fused<<<256, 512, LDS_BYTES, stream>>>(p);
}
```
